```python
import torch

def _softmax(x, axis=-1):
    return torch.softmax(x, dim=axis)
def _gelu(x):
    return torch.nn.functional.gelu(x, approximate="tanh")
def _silu(x):
    return torch.nn.functional.silu(x)
def _sigmoid(x):
    return torch.sigmoid(x)
def _layernorm(x, axis=-1, eps=1e-5):
    m = x.mean(dim=axis, keepdim=True)
    v = x.var(dim=axis, keepdim=True, unbiased=False)
    return (x - m) / torch.sqrt(v + eps)
def _scatter_add(tgt, idx, src, axis=0):
    return tgt.clone().index_add_(axis, idx, src)
def _segment_sum(src, seg, n):
    out = torch.zeros((n,) + src.shape[1:], dtype=src.dtype)
    return out.index_add_(0, seg, src)
def _pad(x, pw):
    flat = tuple(v for pair in reversed(pw) for v in pair)
    return torch.nn.functional.pad(x, flat)

kernel_name = 'interleave_layers__gqa_block_cached__glu_gated_reglu__decode'

def setup_inputs():

    _w = (lambda a: (lambda t: t.to(torch.bfloat16) if t.is_floating_point() else t)(torch.as_tensor(a)))
    def _A_setup():
        B, Sq, D = 1, 1, 576
        Hq, Hkv, Sk, Dh = 32, 16, 65536, 128
        assert Hq % Hkv == 0, (Hq, Hkv)
        return {
            "x": _w((lambda *s: torch.randn(*s, dtype=torch.bfloat16) * 0.05)(B, Sq, D)),
            "k_cache": _w((lambda *s: torch.randn(*s, dtype=torch.bfloat16) * 0.8)(B, Hkv, Sk, Dh)),
            "v_cache": _w((lambda *s: torch.randn(*s, dtype=torch.bfloat16) * 1.6)(B, Hkv, Sk, Dh)),
            "wq": _w((lambda *s: torch.randn(*s, dtype=torch.bfloat16) * 0.8)(D, Hq * Dh) * (D ** -0.5)),
            "wo": _w((lambda *s: torch.randn(*s, dtype=torch.bfloat16) * 1.6)(Hq * Dh, D) * ((Hq * Dh) ** -0.5)),
        }
    _A_in = _A_setup()

    def _B_setup():
        B, S, D, F = 1, 1, 576, 4608
        return {
            "x": _w((lambda *s: torch.randn(*s, dtype=torch.bfloat16) * 0.05)(B, S, D)),
            "W_gate": _w((lambda *s: torch.randn(*s, dtype=torch.bfloat16) * 0.4)(D, F) * (2.0 / D) ** 0.5),
            "W_up": _w((lambda *s: torch.randn(*s, dtype=torch.bfloat16) * 0.4)(D, F) * (2.0 / D) ** 0.5),
            "W_down": _w((lambda *s: torch.randn(*s, dtype=torch.bfloat16) * 0.4)(F, D) * (1.0 / F) ** 0.5),
        }
    _B_in = _B_setup()

    return {
        "A_x": _A_in["x"],
        "A_k_cache": _A_in["k_cache"],
        "A_v_cache": _A_in["v_cache"],
        "A_wq": _A_in["wq"],
        "A_wo": _A_in["wo"],
        "B_x": _B_in["x"],
        "B_W_gate": _B_in["W_gate"],
        "B_W_up": _B_in["W_up"],
        "B_W_down": _B_in["W_down"],
    }

def reference(A_x, A_k_cache, A_v_cache, A_wq, A_wo, B_x, B_W_gate, B_W_up, B_W_down):
    def _A_ref(x, k_cache, v_cache, wq, wo):
        B, Sq, D = x.shape
        Hq, Dh = 32, 128
        Hkv = k_cache.shape[1]
        rep = Hq // Hkv
        q = (lambda x, *d: x.permute(*d))((x @ wq).reshape(B, Sq, Hq, Dh), 0, 2, 1, 3)
        k = (lambda x, n, axis: torch.repeat_interleave(x, n, dim=axis))(k_cache, rep, axis=1)
        v = (lambda x, n, axis: torch.repeat_interleave(x, n, dim=axis))(v_cache, rep, axis=1)
        scores = torch.einsum("bhqd,bhkd->bhqk", q, k) * (Dh ** -0.5)
        p = _softmax(scores, axis=-1)
        o = torch.einsum("bhqk,bhkd->bhqd", p, v)
        return (lambda x, *d: x.permute(*d))(o, 0, 2, 1, 3).reshape(B, Sq, Hq * Dh) @ wo

    def _B_ref(x, W_gate, W_up, W_down):
        gate = torch.einsum("bsd,df->bsf", x, W_gate)
        up = torch.einsum("bsd,df->bsf", x, W_up)
        gate = gate * (lambda x, dt: x.to(dt))(gate > 0, gate.dtype)
        h = gate * up
        return torch.einsum("bsf,fd->bsd", h, W_down)

    x = A_x
    x = x + _A_ref(_layernorm(x, axis=-1), A_k_cache, A_v_cache, A_wq, A_wo)
    x = x + _B_ref(_layernorm(x, axis=-1), B_W_gate, B_W_up, B_W_down)
    x = x + _A_ref(_layernorm(x, axis=-1), A_k_cache, A_v_cache, A_wq, A_wo)
    x = x + _B_ref(_layernorm(x, axis=-1), B_W_gate, B_W_up, B_W_down)
    return x
```

```cpp
#include <hip/hip_runtime.h>
#include <hip/hip_bf16.h>

typedef unsigned short u16;
typedef unsigned int u32;
typedef unsigned long long u64;
typedef __bf16 bf16x8 __attribute__((ext_vector_type(8)));
typedef __bf16 bf16x2 __attribute__((ext_vector_type(2)));
typedef short s16x4 __attribute__((ext_vector_type(4)));
typedef float f32x4 __attribute__((ext_vector_type(4)));
typedef float f32x2 __attribute__((ext_vector_type(2)));
typedef u32 u32x4 __attribute__((ext_vector_type(4)));
typedef u32 u32x2 __attribute__((ext_vector_type(2)));
typedef __amdgpu_buffer_rsrc_t rsrc_t;
#define DEV __device__ __forceinline__
#define LAS __attribute__((address_space(3)))
#define GAS __attribute__((address_space(1)))

#define NR 6
#define ABL 0
#define STAMPS 0
#define DLY_B 8
#define DLY_H 6
#define DLY_Q 6

constexpr int D = 576, HQ = 32, HKV = 16, SK = 65536, DH = 128, FF = 4608;
constexpr int NWG = 256;
constexpr int NSTEP = 32;
constexpr u32 TSTEP = 32768;
constexpr u32 SENT = 0xFFFFFFFFu;
constexpr u32 OOB = 0x7FFFFF00u;
constexpr u32 SPIN_CAP = 1u << 18;
static_assert(3 * NR <= NSTEP - NR, "resident tiles are spread over the pass");

constexpr u32 WS_EPOCH = 0, WS_FLAGS = 4096, WS_ARENA = 8192;
constexpr u32 A_Q = 0;
constexpr u32 A_G = A_Q + 2 * 256 * 256;
constexpr u32 A_P1 = A_G + 2 * 216 * 512;
constexpr u32 A_P2 = A_P1 + 2 * 256 * 1152;
constexpr u32 A_END = A_P2 + 4 * 144 * 256;
constexpr u32 ARENA_BYTES = NWG * 5120;
static_assert(A_END <= ARENA_BYTES, "arena");
constexpr u32 WS_STAMP = WS_ARENA + ARENA_BYTES;
constexpr u32 WS_BYTES = WS_STAMP + NWG * 4 * 32 * 8;

constexpr u32 L_RING = 0;
constexpr u32 L_R1 = 65536;
constexpr u32 L_R2 = L_R1 + 49152;
constexpr u32 L_XV = L_R2 + 36864;
constexpr u32 L_ONES = L_XV + 1152;
constexpr u32 L_CMB = L_ONES + 384;
constexpr u32 L_CML = L_CMB + 3072;
constexpr u32 L_PP = L_CML + 64;
constexpr u32 L_HB = L_PP + 768;
constexpr u32 L_QB = L_HB + 576;
constexpr u32 L_END = L_QB + 512;
static_assert(L_END + 512 <= 163840, "lds");

struct Params {
  const u16* x; const u16* k; const u16* v; const u16* wq; const u16* wo;
  const u16* wg; const u16* wu; const u16* wd; u16* out; unsigned char* ws;
};

DEV u32 rfl(u32 x) { return __builtin_amdgcn_readfirstlane(x); }
DEV rsrc_t mk_rsrc(const void* p, u32 bytes) { return __builtin_amdgcn_make_buffer_rsrc((void*)p, 0, bytes, 0x00020000); }
DEV u32x4 lds128(u32 a) { return *(const LAS u32x4*)(size_t)a; }
DEV u32x2 lds64(u32 a) { return *(const LAS u32x2*)(size_t)a; }
DEV u32 lds32(u32 a) { return *(const LAS u32*)(size_t)a; }
DEV float ldsf(u32 a) { return *(const LAS float*)(size_t)a; }
DEV void sts128(u32 a, u32x4 v) { *(LAS u32x4*)(size_t)a = v; }
DEV void sts64(u32 a, u32x2 v) { *(LAS u32x2*)(size_t)a = v; }
DEV void sts32(u32 a, u32 v) { *(LAS u32*)(size_t)a = v; }
DEV s16x4 ldstr(u32 a) { return __builtin_amdgcn_ds_read_tr16_b64_v4i16((LAS s16x4*)(size_t)a); }
DEV bf16x8 cat8(s16x4 lo, s16x4 hi) {
  typedef short s16x8 __attribute__((ext_vector_type(8)));
  s16x8 r = __builtin_shufflevector(lo, hi, 0, 1, 2, 3, 4, 5, 6, 7);
  return __builtin_bit_cast(bf16x8, r);
}
DEV f32x4 mfma32(bf16x8 a, bf16x8 b, f32x4 c) { return __builtin_amdgcn_mfma_f32_16x16x32_bf16(a, b, c, 0, 0, 0); }
DEV f32x4 mfma16(s16x4 a, s16x4 b, f32x4 c) { return __builtin_amdgcn_mfma_f32_16x16x16bf16_1k(a, b, c, 0, 0, 0); }
DEV u32 pk2(float lo, float hi) { f32x2 v = {lo, hi}; return __builtin_bit_cast(u32, __builtin_convertvector(v, bf16x2)); }
DEV float bflo(u32 w) { return __builtin_bit_cast(float, w << 16); }
DEV float bfhi(u32 w) { return __builtin_bit_cast(float, w & 0xffff0000u); }
DEV float rb(float f) { return bflo(pk2(f, f)); }
DEV u32 canon(u32 w) { return w == SENT ? 0x7FFFFFFFu : w; }
DEV u32 fbits(float f) { return __builtin_bit_cast(u32, f); }
DEV float bitsf(u32 u) { return __builtin_bit_cast(float, u); }
DEV u32x4 canon4(f32x4 a) { return u32x4{canon(fbits(a[0])), canon(fbits(a[1])), canon(fbits(a[2])), canon(fbits(a[3]))}; }
DEV float max2(float a, float b) { float r; asm("v_max_f32_e32 %0, %1, %2" : "=v"(r) : "v"(a), "v"(b)); return r; }
DEV float max3(float a, float b, float c) { float r; asm("v_max3_f32 %0, %1, %2, %3" : "=v"(r) : "v"(a), "v"(b), "v"(c)); return r; }
template <int CTRL> DEV float dpp(float x) { return bitsf(__builtin_amdgcn_mov_dpp(fbits(x), CTRL, 0xf, 0xf, true)); }
DEV float row_sum(float x) { x += dpp<0x128>(x); x += dpp<0x124>(x); x += dpp<0x122>(x); x += dpp<0x121>(x); return x; }
DEV float row_max(float x) { x = fmaxf(x, dpp<0x128>(x)); x = fmaxf(x, dpp<0x124>(x)); x = fmaxf(x, dpp<0x122>(x)); x = fmaxf(x, dpp<0x121>(x)); return x; }
DEV float xrow_sum(float x) {
  auto s = __builtin_amdgcn_permlane16_swap(fbits(x), fbits(x), false, false);
  x = bitsf(s[0]) + bitsf(s[1]);
  auto t = __builtin_amdgcn_permlane32_swap(fbits(x), fbits(x), false, false);
  return bitsf(t[0]) + bitsf(t[1]);
}
DEV float xrow_max(float x) {
  auto s = __builtin_amdgcn_permlane16_swap(fbits(x), fbits(x), false, false);
  x = max2(bitsf(s[0]), bitsf(s[1]));
  auto t = __builtin_amdgcn_permlane32_swap(fbits(x), fbits(x), false, false);
  return max2(bitsf(t[0]), bitsf(t[1]));
}
DEV float wave_sum(float x) { return xrow_sum(row_sum(x)); }
DEV void bar() { asm volatile("s_waitcnt lgkmcnt(0)\n\ts_barrier" ::: "memory"); }
template <int N> DEV void wait_vm() { asm volatile("s_waitcnt vmcnt(%0)" ::"i"(N) : "memory"); }
DEV u32 umax(u32 a, u32 b) { return a > b ? a : b; }
DEV u32 umax4(u32x4 v) { return umax(umax(v.x, v.y), umax(v.z, v.w)); }
DEV u64 pend4(u32x4 v) { return __builtin_amdgcn_ballot_w64(umax4(v) == SENT); }
DEV bool any_lane(bool b) { return __builtin_amdgcn_ballot_w64(b) != 0; }
DEV u32x4 ld_sc1(rsrc_t r, u32 off) { return __builtin_amdgcn_raw_buffer_load_b128(r, off, 0, 16); }
DEV void st_sc1(rsrc_t r, u32 off, u32x4 v) { __builtin_amdgcn_raw_buffer_store_b128(v, r, off, 0, 16); }
DEV void st_sc1_64(rsrc_t r, u32 off, u32x2 v) { __builtin_amdgcn_raw_buffer_store_b64(v, r, off, 0, 16); }
DEV void st_sc1_32(rsrc_t r, u32 off, u32 v) { __builtin_amdgcn_raw_buffer_store_b32(v, r, off, 0, 16); }
DEV void poll_pause() { asm volatile("s_sleep 1" ::: "memory"); }
template <int N> DEV void delay() {
#pragma unroll
  for (int i = 0; i < N / 8; ++i) asm volatile("s_sleep 8" ::: "memory");
  if (N % 8) asm volatile("s_sleep %0" ::"i"(N % 8) : "memory");
}
#define PL(op, d, a, so, off, m) "s_mov_b64 exec, " m "\n\t" op " " d ", " a ", %[r], " so " offen offset:" off " sc1\n\t"
#define PL_END "s_mov_b64 exec, -1\n\ts_waitcnt vmcnt(0)"
constexpr u32x4 SENT4 = {SENT, SENT, SENT, SENT};

DEV void dma4k(rsrc_t r, u32 v0, u32 v1, u32 so, u32 lds) {
  asm volatile(
      "s_waitcnt lgkmcnt(0)\n\t"
      "s_mov_b32 m0, %[l]\n\t"
      "s_nop 0\n\t"
      "buffer_load_dwordx4 %[a], %[r], %[s] offen nt lds\n\t"
      "buffer_load_dwordx4 %[b], %[r], %[s] offen offset:1024 nt lds\n\t"
      "buffer_load_dwordx4 %[a], %[r], %[s] offen offset:2048 nt lds\n\t"
      "buffer_load_dwordx4 %[b], %[r], %[s] offen offset:3072 nt lds"
      :: [a] "v"(v0), [b] "v"(v1), [r] "s"(r), [s] "s"(so), [l] "s"(lds) : "memory");
}
DEV void dma1k(rsrc_t r, u32 voff, u32 so, u32 lds) {
  asm volatile("s_mov_b32 m0, %[l]\n\ts_nop 0\n\tbuffer_load_dwordx4 %[a], %[r], %[s] offen nt lds"
               :: [a] "v"(voff), [r] "s"(r), [s] "s"(so), [l] "s"(lds) : "memory");
}
struct Tile { bf16x8 k[8]; s16x4 v[16]; };
struct Stream { rsrc_t rK, rV; u32 dv0, dv1, slot, ka, va; };
DEV void issue_tile(const Stream& s, u32 so) {
  dma4k(s.rK, s.dv0, s.dv1, so, s.slot);
  dma4k(s.rK, s.dv0, s.dv1, so + 4096, s.slot + 4096);
  dma4k(s.rV, s.dv0, s.dv1, so, s.slot + 8192);
  dma4k(s.rV, s.dv0, s.dv1, so + 4096, s.slot + 12288);
}
template <int W> DEV void move_tile(const Stream& s, Tile& t, u32 so, bool more) {
#pragma unroll
  for (int h = 0; h < 2; ++h) {
    if (more) wait_vm<W>(); else wait_vm<0>();
#pragma unroll
    for (int m = 0; m < 4; ++m) t.k[4 * h + m] = __builtin_bit_cast(bf16x8, lds128((s.ka ^ (m << 6)) + h * 4096));
    if (more) dma4k(s.rK, s.dv0, s.dv1, so + h * 4096, s.slot + h * 4096);
  }
#pragma unroll
  for (int h = 0; h < 2; ++h) {
    if (more) wait_vm<W>();
#pragma unroll
    for (int m = 0; m < 8; ++m) t.v[2 * m + h] = ldstr((s.va ^ (m << 5)) + h * 4096);
    if (more) dma4k(s.rV, s.dv0, s.dv1, so + h * 4096, s.slot + 8192 + h * 4096);
  }
}

struct Soft { f32x4 acc; float m, l; };
constexpr float SCALE_LOG2E = 0.08838834764831845f * 1.4426950408889634f;
DEV void attend(const Tile& t, const bf16x8 (&q)[4], int jm, Soft& st) {
  f32x4 s0 = {0.f, 0.f, 0.f, 0.f}, s1 = {0.f, 0.f, 0.f, 0.f};
#pragma unroll
  for (int m = 0; m < 4; ++m) { s0 = mfma32(t.k[m], q[m], s0); s1 = mfma32(t.k[4 + m], q[m], s1); }
  const float mt = xrow_max(max3(max3(s0[0], s0[1], s0[2]), max3(s0[3], s1[0], s1[1]), max2(s1[2], s1[3]))) * SCALE_LOG2E;
  float p[8];
#pragma unroll
  for (int e = 0; e < 4; ++e) {
    p[e] = __builtin_amdgcn_exp2f(fmaf(s0[e], SCALE_LOG2E, -mt));
    p[4 + e] = __builtin_amdgcn_exp2f(fmaf(s1[e], SCALE_LOG2E, -mt));
  }
  const u32x4 pp = {pk2(p[0], p[1]), pk2(p[2], p[3]), pk2(p[4], p[5]), pk2(p[6], p[7])};
  f32x4 o = {0.f, 0.f, 0.f, 0.f};
#pragma unroll
  for (int m = 0; m < 8; ++m) {
    const bool on = jm == m;
    const u32x4 b = {on ? pp.x : 0u, on ? pp.y : 0u, on ? pp.z : 0u, on ? pp.w : 0u};
    o = mfma32(cat8(t.v[2 * m], t.v[2 * m + 1]), __builtin_bit_cast(bf16x8, b), o);
  }
  const float mn = max2(st.m, mt);
  const float fa = __builtin_amdgcn_exp2f(st.m - mn), fb = __builtin_amdgcn_exp2f(mt - mn);
  st.l = fmaf(st.l, fa, (((p[0] + p[1]) + (p[2] + p[3])) + ((p[4] + p[5]) + (p[6] + p[7]))) * fb);
  st.m = mn;
#pragma unroll
  for (int e = 0; e < 4; ++e) st.acc[e] = fmaf(st.acc[e], fa, o[e] * fb);
}

#define STAMP(i) do { } while (0)
#define STAMP_DUMP() do { } while (0)
#define STAMP_INIT() do { } while (0)

DEV int lane_id() { int l; asm volatile("v_mbcnt_lo_u32_b32 %0, -1, 0\n\tv_mbcnt_hi_u32_b32 %0, -1, %0" : "=v"(l)); __builtin_assume(l >= 0 && l < 64); return l; }
typedef const __attribute__((address_space(4))) Params* kargs_t;
DEV kargs_t kargs() { kargs_t q = (kargs_t)__builtin_amdgcn_kernarg_segment_ptr(); asm volatile("" : "+s"(q)); return q; }
#define PHASE() \
  asm volatile("" : "+s"(wave), "+s"(b), "+s"(layer), "+s"(ws)); \
  const int lane = lane_id(), g = lane >> 4, j = lane & 15; \
  const int hk = b >> 4, sp = b & 15; \
  const bool both = sp == 7 || sp == 8; \
  const int gu = both ? 1000 : hk * 14 + (sp < 7 ? sp : sp - 2); \
  const rsrc_t rA = mk_rsrc(ws + WS_ARENA, ARENA_BYTES); \
  (void)g; (void)j; (void)hk; (void)sp; (void)both; (void)gu; (void)rA

template <int NPIECE> DEV void load_slice(const u16* w, u32 wbytes, u32 rowbytes, u32 off0, u32 lds, int wave, int lane) {
  const rsrc_t r = mk_rsrc(w, wbytes);
  const u32 vo = (lane >> 3) * rowbytes + (((lane & 7) ^ (((lane >> 4) & 3) << 1)) << 4);
#pragma unroll
  for (int i = 0; i < (NPIECE + 3) / 4; ++i)
    if (4 * i + 3 < NPIECE || 4 * i + wave < NPIECE) dma1k(r, vo, off0 + (4 * i + wave) * 8 * rowbytes, lds + (4 * i + wave) * 1024);
}
DEV void load_wq(const u16* wq, int b, bool both, int wave, int lane) {
  load_slice<18>(wq, D * HQ * DH * 2, HQ * DH * 2, (b & 3) * 144 * (HQ * DH * 2) + (b >> 2) * 128, both ? L_R2 : L_R1, wave, lane);
}
DEV void load_wo(const u16* wo, int b, int wave, int lane) {
  load_slice<32>(wo, HQ * DH * D * 2, D * 2, (b >> 4) * 256 * (D * 2) + (b & 15) * 128, L_R2, wave, lane);
}
DEV void load_gu(const u16* w, int gu, u32 lds, int wave, int lane) {
  load_slice<24>(w, D * FF * 2, FF * 2, (gu % 3) * 192 * (FF * 2) + (gu / 3) * 128, lds, wave, lane);
}
DEV void load_alias(int wave, int b) {
  asm volatile("" : "+s"(wave), "+s"(b));
  const int lane = lane_id(), hk = b >> 4, sp = b & 15;
  const int gu = hk * 14 + (sp < 7 ? sp : sp - 2);
  if (sp == 7 || sp == 8) load_wo(kargs()->wo, b, wave, lane);
  else if (gu < 216) load_gu(kargs()->wg, gu, L_R1, wave, lane);
}
DEV u32 slice_addr(u32 base, u32 row, int j) { return base + row * 128 + (((row >> 1) & 3) << 5) + (j & 3) * 8; }
DEV void ln_stats(int g, int j, float& mb, float& inv) {
  const u32x4 va_ = lds128(L_XV + j * 64 + g * 16), vb_ = lds128(L_XV + 1024 + (j & 1) * 64 + g * 16);
  const float c = bflo(lds32(L_XV));
  float s = 0.f, q = 0.f, s2 = 0.f, q2 = 0.f;
#pragma unroll
  for (int e = 0; e < 4; ++e) {
    const float a0 = bflo(va_[e]) - c, a1 = bfhi(va_[e]) - c, b0 = bflo(vb_[e]) - c, b1 = bfhi(vb_[e]) - c;
    s += a0 + a1; q = fmaf(a0, a0, fmaf(a1, a1, q));
    s2 += b0 + b1; q2 = fmaf(b0, b0, fmaf(b1, b1, q2));
  }
  if (j < 2) { s += s2; q += q2; }
  const float ms = wave_sum(s) * (1.0f / D), mq = wave_sum(q) * (1.0f / D);
  const float var = fmaxf(mq - ms * ms, 0.f);
  mb = rb(c + ms);
  inv = __builtin_amdgcn_rcpf(rb(__builtin_amdgcn_sqrtf(rb(rb(var) + 1e-5f))));
}
DEV void publish_cols(rsrc_t rA, u32 rec, f32x4 a, int wave, int g, int j) {
  if (wave != 0 && j < 4) sts128(L_PP + (wave - 1) * 256 + j * 64 + g * 16, __builtin_bit_cast(u32x4, a));
  bar();
  if (wave == 0 && j < 4) {
#pragma unroll
    for (int w = 0; w < 3; ++w) a += __builtin_bit_cast(f32x4, lds128(L_PP + w * 256 + j * 64 + g * 16));
    st_sc1(rA, rec + j * 64 + g * 16, canon4(a));
  }
}
DEV void gather_cols(rsrc_t rA, int use, bool final_out, int wave, int g, int j) {
  const u32 base = A_P2 + use * (144 * 256);
  const u32 p0 = 32 * wave + j, p1 = p0 + 16, p2 = 128 + 4 * wave + (j & 3);
  const u32 o0 = base + g * 2304 + wave * 512 + j * 16;
  const u32 o2 = base + (4 * g + (j >> 2)) * 2304 + 2048 + wave * 64 + (j & 3) * 16;
  const u32 pm = g == 0 ? p0 : g == 1 ? p1 : p2;
  const u32x2 xr = lds64(L_XV + pm * 8);
  u32x4 t[9];
#pragma unroll
  for (int i = 0; i < 9; ++i) t[i] = SENT4;
  delay<DLY_B>();
  _Pragma("nounroll") for (u32 spin = 0; spin < SPIN_CAP; ++spin) {
    u64 m[9];
#pragma unroll
    for (int i = 0; i < 9; ++i) m[i] = pend4(t[i]);
    if (((m[0] | m[1]) | (m[2] | m[3]) | (m[4] | m[5]) | (m[6] | m[7]) | m[8]) == 0) break;
    if (spin) poll_pause();
    asm volatile(PL("buffer_load_dwordx4", "%0", "%[a]", "0", "0", "%[m0]") PL("buffer_load_dwordx4", "%1", "%[a]", "%[s1]", "0", "%[m1]")
                 PL("buffer_load_dwordx4", "%2", "%[a]", "%[s2]", "0", "%[m2]") PL("buffer_load_dwordx4", "%3", "%[a]", "%[s3]", "0", "%[m3]")
                 PL("buffer_load_dwordx4", "%4", "%[a]", "0", "256", "%[m4]") PL("buffer_load_dwordx4", "%5", "%[a]", "%[s1]", "256", "%[m5]")
                 PL("buffer_load_dwordx4", "%6", "%[a]", "%[s2]", "256", "%[m6]") PL("buffer_load_dwordx4", "%7", "%[a]", "%[s3]", "256", "%[m7]")
                 PL("buffer_load_dwordx4", "%8", "%[c]", "0", "0", "%[m8]") PL_END
                 : "+v"(t[0]), "+v"(t[1]), "+v"(t[2]), "+v"(t[3]), "+v"(t[4]), "+v"(t[5]), "+v"(t[6]), "+v"(t[7]), "+v"(t[8])
                 : [a] "v"(o0), [c] "v"(o2), [r] "s"(rA), [s1] "s"(9216), [s2] "s"(18432), [s3] "s"(27648),
                   [m0] "s"(m[0]), [m1] "s"(m[1]), [m2] "s"(m[2]), [m3] "s"(m[3]), [m4] "s"(m[4]), [m5] "s"(m[5]), [m6] "s"(m[6]),
                   [m7] "s"(m[7]), [m8] "s"(m[8])
                 : "memory");
  }
  float s[4];
#pragma unroll
  for (int e = 0; e < 4; ++e) {
    const float a0 = (bitsf(t[0][e]) + bitsf(t[1][e])) + (bitsf(t[2][e]) + bitsf(t[3][e]));
    const float a1 = (bitsf(t[4][e]) + bitsf(t[5][e])) + (bitsf(t[6][e]) + bitsf(t[7][e]));
    float a2 = bitsf(t[8][e]);
    a2 += dpp<0x124>(a2); a2 += dpp<0x128>(a2);
    auto x = __builtin_amdgcn_permlane16_swap(fbits(a0), fbits(a1), false, false);
    const float c01 = bitsf(x[0]) + bitsf(x[1]);
    auto y = __builtin_amdgcn_permlane16_swap(fbits(a2), fbits(a2), false, false);
    const float c2 = bitsf(y[0]) + bitsf(y[1]);
    auto z = __builtin_amdgcn_permlane32_swap(fbits(c01), fbits(c2), false, false);
    s[e] = bitsf(z[0]) + bitsf(z[1]);
  }
  const u32x2 xo = {pk2(bflo(xr.x) + rb(s[0]), bfhi(xr.x) + rb(s[1])), pk2(bflo(xr.y) + rb(s[2]), bfhi(xr.y) + rb(s[3]))};
  const bool mine = g < 2 || (g == 2 && j < 4);
  if (final_out) {
    GAS u32x2* op = (GAS u32x2*)kargs()->out;
    if (mine) op[pm] = xo;
  } else if (mine) {
    sts64(L_XV + pm * 8, xo);
  }
}
#define STREAM(PK, PV) \
  Stream sr; \
  sr.rK = mk_rsrc((PK) + (size_t)hk * SK * DH, SK * DH * 2); \
  sr.rV = mk_rsrc((PV) + (size_t)hk * SK * DH, SK * DH * 2); \
  sr.dv0 = g * 256 + ((j ^ (g << 1)) << 4); sr.dv1 = sr.dv0 ^ 128;                \
  sr.slot = L_RING + wave * 16384; \
  sr.ka = sr.slot + j * 256 + ((g ^ ((j & 7) << 1)) << 4);                        \
  sr.va = sr.slot + 8192 + (4 * g + (j >> 2)) * 256 + ((((4 * g + (j >> 2)) & 7)) << 5) + (j & 3) * 8; \
  const u32 tile0 = sp * (1u << 20) + wave * 8192
#define MOVE_TILES(T0, T1) \
  if (layer == 0) { \
    PHASE(); \
    STREAM(kargs()->k, kargs()->v); \
    _Pragma("unroll") for (int t = (T0); t < (T1); ++t) move_tile<12>(sr, st[t], tile0 + (t + 1) * TSTEP, true); \
  }

__global__ void __launch_bounds__(256, 1) fused_decode(Params p) {
  __shared__ __attribute__((aligned(256))) unsigned char smem[163840];
  asm volatile("" ::"v"((u32)(size_t)(LAS unsigned char*)smem));
  int wave = rfl(threadIdx.x >> 6);
  int b = blockIdx.x;
  int layer = 0;
  unsigned char* ws = p.ws;
  STAMP_INIT();
  STAMP(0);

  u32 ep_lo, ep_hi;
  Tile st[NR];
  {
    PHASE();
    STREAM(p.k, p.v);
    issue_tile(sr, tile0);
    STAMP(25);
    u32x2 ep; u32x4 xa;
    {
      const rsrc_t rX = mk_rsrc(p.x, D * 2);
      const rsrc_t rE = mk_rsrc(ws + WS_EPOCH, 8);
      const u32 o1 = (wave * 64 + lane) * 16;
      asm volatile("buffer_load_dwordx2 %0, off, %2, 0 sc1\n\t"
                   "buffer_load_dwordx4 %1, %3, %4, 0 offen nt"
                   : "=&v"(ep), "=&v"(xa) : "s"(rE), "v"(o1), "s"(rX) : "memory");
    }
    load_wq(p.wq, b, both, wave, lane);
    st_sc1(rA, b * 5120 + wave * 1024 + lane * 16, SENT4);
    if (wave == 0) st_sc1(rA, b * 5120 + 4096 + lane * 16, SENT4);
    if (sp < 7) load_wo(p.wo, b, wave, lane);
    else load_slice<36>(p.wd, FF * D * 2, D * 2, hk * 288 * (D * 2) + (sp - 7) * 128, both ? L_R1 : L_R2, wave, lane);
    if (gu < 216) load_gu(p.wu, gu, L_R1 + 24576, wave, lane);
    if (wave == 3 && lane < 24) sts128(L_ONES + lane * 16, u32x4{0x3F803F80u, 0x3F803F80u, 0x3F803F80u, 0x3F803F80u});
    asm volatile("s_waitcnt vmcnt(8)" : "+v"(ep), "+v"(xa) :: "memory");
    move_tile<63>(sr, st[0], tile0 + TSTEP, true);
    if (wave * 64 + lane < 72) sts128(L_XV + (wave * 64 + lane) * 16, xa);
    const u64 e1 = (((u64)rfl(ep.y) << 32) | rfl(ep.x)) + 1;
    ep_lo = (u32)e1; ep_hi = (u32)(e1 >> 32);
  }

  for (;;) {
    f32x4 qp;
    {
      PHASE();
      STAMP(layer == 0 ? 1 : 13);
      if (layer == 1) wait_vm<0>();
      bar();
      GAS u64* fl_ptr = (GAS u64*)(ws + WS_FLAGS) + b;
      if (layer == 0 && wave == 0 && lane == 0)
        __hip_atomic_store(fl_ptr, ((u64)ep_hi << 32) | ep_lo, __ATOMIC_RELAXED, __HIP_MEMORY_SCOPE_AGENT);
      f32x4 aq = {0.f, 0.f, 0.f, 0.f};
      const u32 wb = both ? L_R2 : L_R1;
      const u32 ta = slice_addr(wb, 8 * g + (j >> 2), j) ^ (wave << 5), tb = slice_addr(wb, 8 * g + 4 + (j >> 2), j) ^ (wave << 5);
      const u32 xn = j == 1 ? L_ONES : L_XV + (b & 3) * 288;
#pragma unroll
      for (int i = 0; i < 4; ++i)
        aq = mfma32(cat8(ldstr(ta + i * 4096), ldstr(tb + i * 4096)), __builtin_bit_cast(bf16x8, lds128(xn + i * 64 + g * 16)), aq);
      aq = mfma16(ldstr(slice_addr(wb, 128 + 4 * g + (j >> 2), j) ^ (wave << 5)), __builtin_bit_cast(s16x4, lds64(xn + 256 + g * 8)), aq);
      float mb, inv;
      ln_stats(g, j, mb, inv);
#pragma unroll
      for (int e = 0; e < 4; ++e) qp[e] = (aq[e] - mb * dpp<0xB1>(aq[e])) * inv;
    }
    MOVE_TILES(1, (NR + 1) / 2);
    {
      PHASE();
      if (layer == 0) {
        const rsrc_t rF = mk_rsrc(ws + WS_FLAGS, NWG * 8);
        _Pragma("nounroll") for (u32 spin = 0; spin < SPIN_CAP; ++spin) {
          const u32x4 f0 = ld_sc1(rF, lane * 32), f1 = ld_sc1(rF, lane * 32 + 16);
          const u32 bad = ((f0.x ^ ep_lo) | (f0.y ^ ep_hi)) | ((f0.z ^ ep_lo) | (f0.w ^ ep_hi)) | ((f1.x ^ ep_lo) | (f1.y ^ ep_hi)) | ((f1.z ^ ep_lo) | (f1.w ^ ep_hi));
          if (!any_lane(bad != 0)) break;
          poll_pause();
        }
      }
      if (j == 0) st_sc1(rA, A_Q + (layer * 256 + b) * 256 + wave * 64 + g * 16, canon4(qp));
      STAMP(layer == 0 ? 2 : 14);
    }
    MOVE_TILES((NR + 1) / 2, NR);

    Soft sm;
    {
      PHASE();
      STREAM(kargs()->k, kargs()->v);
      const int jm = j >> 1;
      STAMP(layer == 0 ? 3 : 15);
      bf16x8 q[4];
      {
        const u32 off = A_Q + (layer * 256 + (4 * hk + wave) * 4 + g) * 256 + j * 16;
        u32x4 t = SENT4;
        delay<DLY_Q>();
        _Pragma("nounroll") for (u32 spin = 0; spin < SPIN_CAP; ++spin) {
          const u64 m0 = pend4(t);
          if (m0 == 0) break;
          if (spin) poll_pause();
          asm volatile(PL("buffer_load_dwordx4", "%0", "%[a]", "0", "0", "%[m0]") PL_END : "+v"(t) : [a] "v"(off), [r] "s"(rA), [m0] "s"(m0) : "memory");
        }
        const u32x2 qb = {pk2(xrow_sum(bitsf(t.x)), xrow_sum(bitsf(t.y))), pk2(xrow_sum(bitsf(t.z)), xrow_sum(bitsf(t.w)))};
        if (g == 0) sts64(L_QB + wave * 128 + j * 8, qb);
        bar();
#pragma unroll
        for (int m = 0; m < 4; ++m) q[m] = __builtin_bit_cast(bf16x8, lds128(L_QB + (j & 1) * 256 + m * 64 + g * 16));
      }
      STAMP(layer == 0 ? 4 : 16);

      sm.acc = f32x4{0.f, 0.f, 0.f, 0.f}; sm.m = -INFINITY; sm.l = 0.f;
      u32 nx = tile0 + (layer == 0 ? NR + 1 : NR) * TSTEP;
      const int sphase = b % 3;
      _Pragma("nounroll") for (int it = 0; it < NSTEP - NR; ++it) {
        Tile tl;
        move_tile<12>(sr, tl, nx, it + 1 < NSTEP - NR);
        nx += TSTEP;
        if (it == 0) load_alias(wave, b);
        attend(tl, q, jm, sm);
#pragma unroll
        for (int t = 0; t < NR; ++t)
          if (it == 3 * t + sphase) attend(st[t], q, jm, sm);
        if (it + 1 < NSTEP - NR) asm volatile("s_barrier" ::: "memory");
      }
      STAMP(layer == 0 ? 5 : 17);
    }
    {
      PHASE();
      const float lt = xrow_sum(sm.l);
      if (wave != 0) sts128(L_CMB + (wave - 1) * 1024 + lane * 16, __builtin_bit_cast(u32x4, sm.acc));
      if (lane < 2) sts64(L_CML + wave * 16 + lane * 8, u32x2{fbits(sm.m), fbits(lt)});
      bar();
      if (wave == 0) {
        float mw[4], lw[4];
#pragma unroll
        for (int w = 0; w < 4; ++w) { const u32x2 t = lds64(L_CML + w * 16 + (j & 1) * 8); mw[w] = bitsf(t.x); lw[w] = bitsf(t.y); }
        const float M = max2(max2(mw[0], mw[1]), max2(mw[2], mw[3]));
        float sc[4], L = 0.f;
#pragma unroll
        for (int w = 0; w < 4; ++w) { sc[w] = __builtin_amdgcn_exp2f(mw[w] - M); L = fmaf(sc[w], lw[w], L); }
        f32x4 o = sm.acc * sc[0];
#pragma unroll
        for (int w = 1; w < 4; ++w) o += __builtin_bit_cast(f32x4, lds128(L_CMB + (w - 1) * 1024 + lane * 16)) * sc[w];
        const u32 rec = A_P1 + (layer * 256 + b) * 1152;
        st_sc1(rA, rec + (j & 1) * 512 + (j >> 1) * 64 + g * 16, canon4(o));
        if (lane < 2) st_sc1_64(rA, rec + 1024 + lane * 8, u32x2{canon(fbits(M)), canon(fbits(L))});
      }
      STAMP(layer == 0 ? 6 : 18);
    }
    {
      PHASE();
      if (sp < 9) {
        u32x4 t[4] = {SENT4, SENT4, SENT4, SENT4}; u32x2 ml = {SENT, SENT};
        const u32 rec = A_P1 + (layer * 256 + 16 * hk + j) * 1152;
        const u32 o1 = rec + wave * 256 + g * 16, o2 = rec + 1024 + (wave >> 1) * 8;
        _Pragma("nounroll") for (u32 spin = 0; spin < SPIN_CAP; ++spin) {
          const u64 m0 = pend4(t[0]), m1 = pend4(t[1]), m2 = pend4(t[2]), m3 = pend4(t[3]);
          const u64 m4 = __builtin_amdgcn_ballot_w64(umax(ml.x, ml.y) == SENT);
          if (((m0 | m1) | (m2 | m3) | m4) == 0) break;
          if (spin) poll_pause();
          asm volatile(PL("buffer_load_dwordx2", "%4", "%[b]", "0", "0", "%[m4]")
                       PL("buffer_load_dwordx4", "%0", "%[a]", "0", "0", "%[m0]") PL("buffer_load_dwordx4", "%1", "%[a]", "0", "64", "%[m1]")
                       PL("buffer_load_dwordx4", "%2", "%[a]", "0", "128", "%[m2]") PL("buffer_load_dwordx4", "%3", "%[a]", "0", "192", "%[m3]") PL_END
                       : "+v"(t[0]), "+v"(t[1]), "+v"(t[2]), "+v"(t[3]), "+v"(ml)
                       : [a] "v"(o1), [b] "v"(o2), [r] "s"(rA), [m0] "s"(m0), [m1] "s"(m1), [m2] "s"(m2), [m3] "s"(m3), [m4] "s"(m4) : "memory");
        }
        STAMP(layer == 0 ? 7 : 19);
        const float M = row_max(bitsf(ml.x));
        const float w0 = __builtin_amdgcn_exp2f(bitsf(ml.x) - M);
        const float w = w0 * __builtin_amdgcn_rcpf(row_sum(w0 * bitsf(ml.y)));
        u32 ob[8];
#pragma unroll
        for (int i = 0; i < 4; ++i) {
          ob[2 * i] = pk2(row_sum(w * bitsf(t[i].x)), row_sum(w * bitsf(t[i].y)));
          ob[2 * i + 1] = pk2(row_sum(w * bitsf(t[i].z)), row_sum(w * bitsf(t[i].w)));
        }
        f32x4 a = {0.f, 0.f, 0.f, 0.f};
        const u32 ta = slice_addr(L_R2, 64 * wave + 4 * g + (j >> 2), j);
#pragma unroll
        for (int kk = 0; kk < 2; ++kk)
#pragma unroll
          for (int c = 0; c < 4; ++c) {
            const bool on = j == c;
            const u32x4 bm = {on ? ob[4 * kk] : 0u, on ? ob[4 * kk + 1] : 0u, on ? ob[4 * kk + 2] : 0u, on ? ob[4 * kk + 3] : 0u};
            a = mfma32(cat8(ldstr((ta ^ (c << 5)) + kk * 4096), ldstr((ta ^ (c << 5)) + kk * 4096 + 2048)), __builtin_bit_cast(bf16x8, bm), a);
          }
        publish_cols(rA, A_P2 + ((2 * layer) * 144 + hk * 9 + sp) * 256, a, wave, g, j);
        STAMP(layer == 0 ? 8 : 20);
      }
      gather_cols(rA, 2 * layer, false, wave, g, j);
      STAMP(layer == 0 ? 9 : 21);
    }
    bar();
    {
      PHASE();
      if (gu < 216) {
        f32x4 ag = {0.f, 0.f, 0.f, 0.f}, au = ag;
        const u32 ta = slice_addr(L_R1, 8 * g + (j >> 2), j) ^ (wave << 5), tb = slice_addr(L_R1, 8 * g + 4 + (j >> 2), j) ^ (wave << 5);
        const u32 xn = (j == 1 ? L_ONES : L_XV + (gu % 3) * 384) + g * 16;
#pragma unroll
        for (int i = 0; i < 6; ++i) {
          const bf16x8 bb = __builtin_bit_cast(bf16x8, lds128(xn + i * 64));
          ag = mfma32(cat8(ldstr(ta + i * 4096), ldstr(tb + i * 4096)), bb, ag);
          au = mfma32(cat8(ldstr(ta + 24576 + i * 4096), ldstr(tb + 24576 + i * 4096)), bb, au);
        }
        float mb, inv;
        ln_stats(g, j, mb, inv);
        f32x4 r;
#pragma unroll
        for (int e = 0; e < 4; ++e) {
          const float sg = dpp<0xB1>(ag[e]), su = dpp<0xB1>(au[e]);
          r[e] = ((j == 0 ? ag[e] : su) - mb * (j == 0 ? sg : au[e])) * inv;
        }
        if (j < 2) st_sc1(rA, A_G + (layer * 216 + gu) * 512 + wave * 128 + j * 64 + g * 16, canon4(r));
      }
      STAMP(layer == 0 ? 10 : 22);
    }
    {
      PHASE();
      if (sp >= 7) {
        u32x4 t[4] = {SENT4, SENT4, SENT4, SENT4};
        const u32 u1 = 288 * hk + 64 * wave + 4 * j, u2 = 288 * hk + 256 + 4 * (j & 7);
        const u32 gb = A_G + (layer * 216 + g) * 512;
        const u32 o1 = g < 3 ? gb + (u1 >> 6) * 1536 + ((u1 >> 4) & 3) * 128 + (u1 & 15) * 4 : OOB;
        const u32 o2 = g < 3 && wave == 3 ? gb + (u2 >> 6) * 1536 + ((u2 >> 4) & 3) * 128 + (u2 & 15) * 4 : OOB;
        delay<DLY_H>();
        _Pragma("nounroll") for (u32 spin = 0; spin < SPIN_CAP; ++spin) {
          const u64 m0 = pend4(t[0]), m1 = pend4(t[1]), m2 = pend4(t[2]), m3 = pend4(t[3]);
          if (((m0 | m1) | (m2 | m3)) == 0) break;
          if (spin) poll_pause();
          asm volatile(PL("buffer_load_dwordx4", "%0", "%[a]", "0", "0", "%[m0]") PL("buffer_load_dwordx4", "%1", "%[a]", "0", "64", "%[m1]")
                       PL("buffer_load_dwordx4", "%2", "%[b]", "0", "0", "%[m2]") PL("buffer_load_dwordx4", "%3", "%[b]", "0", "64", "%[m3]") PL_END
                       : "+v"(t[0]), "+v"(t[1]), "+v"(t[2]), "+v"(t[3])
                       : [a] "v"(o1), [b] "v"(o2), [r] "s"(rA), [m0] "s"(m0), [m1] "s"(m1), [m2] "s"(m2), [m3] "s"(m3) : "memory");
        }
        STAMP(layer == 0 ? 11 : 23);
        auto hquad = [&](u32x4 tg, u32x4 tu) {
          float h[4];
#pragma unroll
          for (int e = 0; e < 4; ++e) {
            const float gv = rb(xrow_sum(bitsf(tg[e]))), uv = rb(xrow_sum(bitsf(tu[e])));
            h[e] = (gv * (gv > 0.f ? 1.f : 0.f)) * uv;
          }
          return u32x2{pk2(h[0], h[1]), pk2(h[2], h[3])};
        };
        const u32x2 h1 = hquad(t[0], t[1]);
        if (g == 0) sts64(L_HB + wave * 128 + j * 8, h1);
        if (wave == 3) { const u32x2 h2 = hquad(t[2], t[3]); if (g == 0 && j < 8) sts64(L_HB + 512 + j * 8, h2); }
        f32x4 a = {0.f, 0.f, 0.f, 0.f};
        const u32 wb = both ? L_R1 : L_R2;
        const u32 ta = slice_addr(wb, 64 * wave + 8 * g + (j >> 2), j), tb = slice_addr(wb, 64 * wave + 8 * g + 4 + (j >> 2), j);
#pragma unroll
        for (int kk = 0; kk < 3; ++kk)
          if (kk < 2 || wave == 3) {
            const u32x4 hb = lds128(L_HB + wave * 128 + kk * 64 + g * 16);
#pragma unroll
            for (int c = 0; c < 4; ++c) {
              const bool on = j == c;
              const u32x4 bm = {on ? hb.x : 0u, on ? hb.y : 0u, on ? hb.z : 0u, on ? hb.w : 0u};
              a = mfma32(cat8(ldstr((ta ^ (c << 5)) + kk * 4096), ldstr((tb ^ (c << 5)) + kk * 4096)), __builtin_bit_cast(bf16x8, bm), a);
            }
          }
        publish_cols(rA, A_P2 + ((2 * layer + 1) * 144 + hk * 9 + sp - 7) * 256, a, wave, g, j);
        STAMP(layer == 0 ? 12 : 24);
      } else {
        bar();
      }
      if (layer == 0) load_wq(kargs()->wq, b, both, wave, lane);
      if (layer == 0 || b == 0) gather_cols(rA, 2 * layer + 1, layer == 1, wave, g, j);
      if (layer == 1) {
        GAS u64* ep_ptr = (GAS u64*)(ws + WS_EPOCH);
        if (wave == 0 && b == 0 && lane == 0)
          __hip_atomic_store(ep_ptr, ((u64)ep_hi << 32) | ep_lo, __ATOMIC_RELAXED, __HIP_MEMORY_SCOPE_AGENT);
        STAMP(26);
        STAMP_DUMP();
        break;
      }
      layer = 1;
    }
  }
}

extern "C" void kernel_launch(void* const* d_in, const int* in_sizes, int n_in, void* d_out, int out_size, void* d_ws,
                              size_t ws_size, hipStream_t stream) {
  Params p;
  p.x = (const u16*)d_in[0]; p.k = (const u16*)d_in[1]; p.v = (const u16*)d_in[2];
  p.wq = (const u16*)d_in[3]; p.wo = (const u16*)d_in[4];
  p.wg = (const u16*)d_in[6]; p.wu = (const u16*)d_in[7]; p.wd = (const u16*)d_in[8];
  p.out = (u16*)d_out; p.ws = (unsigned char*)d_ws;
  hipLaunchKernelGGL(fused_decode, dim3(NWG), dim3(256), 0, stream, p);
}
```

```cpp
#include <hip/hip_runtime.h>
#include <hip/hip_bf16.h>

typedef unsigned short u16;
typedef unsigned int u32;
typedef unsigned long long u64;
typedef __bf16 bf16x8 __attribute__((ext_vector_type(8)));
typedef __bf16 bf16x2 __attribute__((ext_vector_type(2)));
typedef short s16x4 __attribute__((ext_vector_type(4)));
typedef float f32x4 __attribute__((ext_vector_type(4)));
typedef float f32x2 __attribute__((ext_vector_type(2)));
typedef u32 u32x4 __attribute__((ext_vector_type(4)));
typedef u32 u32x2 __attribute__((ext_vector_type(2)));
typedef __amdgpu_buffer_rsrc_t rsrc_t;
#define DEV __device__ __forceinline__
#define LAS __attribute__((address_space(3)))
#define GAS __attribute__((address_space(1)))

#define NR 6
#define ABL 0
#define KEEPMASK 0x49249249u
#define NL2 2
#define NS1 23
#define KEEPMUL 1
#define STAMPS 0
#define SLP_L 3
#define H1LIGHT 1
#define X2LIGHT 0
#define DLY_H 55
#define DLY_X2 150
#define DLY_Q 50

constexpr int D = 576, HQ = 32, HKV = 16, SK = 65536, DH = 128, FF = 4608;
constexpr int NWG = 256;
constexpr int NSTEP = 32;
constexpr u32 TSTEP = 32768;
constexpr u32 SENT = 0xFFFFFFFFu;
constexpr u32 OOB = 0x7FFFFF00u;
constexpr u32 SPIN_CAP = 1u << 18;
constexpr u32 NONE = 0xFFFFFFFFu;
static_assert(2 * NR <= NS1 - NR && NS1 <= NSTEP - 1 - NL2, "resident tiles are spread over the static iterations");

constexpr u32 WS_EPOCH = 0, WS_FLAGS = 4096, WS_ARENA = 8192;
constexpr u32 A_CLAIM = 0;
constexpr u32 A_Q = 4096;
constexpr u32 A_G = A_Q + 2 * 256 * 256;
constexpr u32 A_P1 = A_G + 2 * 216 * 512;
constexpr u32 A_P2 = A_P1 + 2 * 256 * 1152;
constexpr u32 A_END = A_P2 + 4 * 144 * 256;
constexpr u32 ARENA_BYTES = NWG * 5120;
static_assert(A_END <= ARENA_BYTES, "arena");
constexpr u32 WS_STAMP = WS_ARENA + ARENA_BYTES;
constexpr u32 WS_BYTES = WS_STAMP + NWG * 4 * 32 * 8;

constexpr u32 L_RING = 0;
constexpr u32 L_R1 = 65536;
constexpr u32 L_R2 = L_R1 + 49152;
constexpr u32 L_XV = L_R2 + 36864;
constexpr u32 L_ONES = L_XV + 1152;
constexpr u32 L_CMB = L_ONES + 384;
constexpr u32 L_CML = L_CMB + 3072;
constexpr u32 L_PP = L_CML + 64;
constexpr u32 L_HB = L_PP + 768;
constexpr u32 L_QB = L_HB + 768;
constexpr u32 L_ST = L_QB + 512;
constexpr u32 L_NX = L_ST + 48;
constexpr u32 L_END = L_NX + 16;
static_assert(L_END + 512 <= 163840, "lds");

struct Params {
  const u16* x; const u16* k; const u16* v; const u16* wq; const u16* wo;
  const u16* wg; const u16* wu; const u16* wd; u16* out; unsigned char* ws;
};

DEV u32 rfl(u32 x) { return __builtin_amdgcn_readfirstlane(x); }
DEV rsrc_t mk_rsrc(const void* p, u32 bytes) { return __builtin_amdgcn_make_buffer_rsrc((void*)p, 0, bytes, 0x00020000); }
DEV u32x4 lds128(u32 a) { return *(const LAS u32x4*)(size_t)a; }
DEV u32x2 lds64(u32 a) { return *(const LAS u32x2*)(size_t)a; }
DEV u32 lds32(u32 a) { return *(const LAS u32*)(size_t)a; }
DEV float ldsf(u32 a) { return *(const LAS float*)(size_t)a; }
DEV void sts128(u32 a, u32x4 v) { *(LAS u32x4*)(size_t)a = v; }
DEV void sts64(u32 a, u32x2 v) { *(LAS u32x2*)(size_t)a = v; }
DEV void sts32(u32 a, u32 v) { *(LAS u32*)(size_t)a = v; }
DEV s16x4 ldstr(u32 a) { return __builtin_amdgcn_ds_read_tr16_b64_v4i16((LAS s16x4*)(size_t)a); }
DEV bf16x8 cat8(s16x4 lo, s16x4 hi) {
  typedef short s16x8 __attribute__((ext_vector_type(8)));
  s16x8 r = __builtin_shufflevector(lo, hi, 0, 1, 2, 3, 4, 5, 6, 7);
  return __builtin_bit_cast(bf16x8, r);
}
DEV f32x4 mfma32(bf16x8 a, bf16x8 b, f32x4 c) { return __builtin_amdgcn_mfma_f32_16x16x32_bf16(a, b, c, 0, 0, 0); }
DEV f32x4 mfma16(s16x4 a, s16x4 b, f32x4 c) { return __builtin_amdgcn_mfma_f32_16x16x16bf16_1k(a, b, c, 0, 0, 0); }
DEV u32 pk2(float lo, float hi) { f32x2 v = {lo, hi}; return __builtin_bit_cast(u32, __builtin_convertvector(v, bf16x2)); }
DEV float bflo(u32 w) { return __builtin_bit_cast(float, w << 16); }
DEV float bfhi(u32 w) { return __builtin_bit_cast(float, w & 0xffff0000u); }
DEV float rb(float f) { return bflo(pk2(f, f)); }
DEV u32 canon(u32 w) { return w == SENT ? 0x7FFFFFFFu : w; }
DEV u32 fbits(float f) { return __builtin_bit_cast(u32, f); }
DEV float bitsf(u32 u) { return __builtin_bit_cast(float, u); }
DEV u32x4 canon4(f32x4 a) { return u32x4{canon(fbits(a[0])), canon(fbits(a[1])), canon(fbits(a[2])), canon(fbits(a[3]))}; }
DEV float max2(float a, float b) { float r; asm("v_max_f32_e32 %0, %1, %2" : "=v"(r) : "v"(a), "v"(b)); return r; }
DEV float max3(float a, float b, float c) { float r; asm("v_max3_f32 %0, %1, %2, %3" : "=v"(r) : "v"(a), "v"(b), "v"(c)); return r; }
template <int CTRL> DEV float dpp(float x) { return bitsf(__builtin_amdgcn_mov_dpp(fbits(x), CTRL, 0xf, 0xf, true)); }
DEV float row_sum(float x) { x += dpp<0x128>(x); x += dpp<0x124>(x); x += dpp<0x122>(x); x += dpp<0x121>(x); return x; }
DEV float row_max(float x) { x = fmaxf(x, dpp<0x128>(x)); x = fmaxf(x, dpp<0x124>(x)); x = fmaxf(x, dpp<0x122>(x)); x = fmaxf(x, dpp<0x121>(x)); return x; }
DEV float xrow_sum(float x) {
  auto s = __builtin_amdgcn_permlane16_swap(fbits(x), fbits(x), false, false);
  x = bitsf(s[0]) + bitsf(s[1]);
  auto t = __builtin_amdgcn_permlane32_swap(fbits(x), fbits(x), false, false);
  return bitsf(t[0]) + bitsf(t[1]);
}
DEV float xrow_max(float x) {
  auto s = __builtin_amdgcn_permlane16_swap(fbits(x), fbits(x), false, false);
  x = max2(bitsf(s[0]), bitsf(s[1]));
  auto t = __builtin_amdgcn_permlane32_swap(fbits(x), fbits(x), false, false);
  return max2(bitsf(t[0]), bitsf(t[1]));
}
DEV float wave_sum(float x) { return xrow_sum(row_sum(x)); }
DEV void bar() { asm volatile("s_waitcnt lgkmcnt(0)\n\ts_barrier" ::: "memory"); }
template <int N> DEV void wait_vm() { asm volatile("s_waitcnt vmcnt(%0)" ::"i"(N) : "memory"); }
DEV u32 umax(u32 a, u32 b) { return a > b ? a : b; }
DEV u32 umax4(u32x4 v) { return umax(umax(v.x, v.y), umax(v.z, v.w)); }
DEV u64 pend4(u32x4 v) { return __builtin_amdgcn_ballot_w64(umax4(v) == SENT); }
DEV bool any_lane(bool b) { return __builtin_amdgcn_ballot_w64(b) != 0; }
DEV u32x4 ld_sc1(rsrc_t r, u32 off) { return __builtin_amdgcn_raw_buffer_load_b128(r, off, 0, 16); }
DEV void st_sc1(rsrc_t r, u32 off, u32x4 v) { __builtin_amdgcn_raw_buffer_store_b128(v, r, off, 0, 16); }
DEV void st_sc1_64(rsrc_t r, u32 off, u32x2 v) { __builtin_amdgcn_raw_buffer_store_b64(v, r, off, 0, 16); }
DEV void st_sc1_32(rsrc_t r, u32 off, u32 v) { __builtin_amdgcn_raw_buffer_store_b32(v, r, off, 0, 16); }
DEV void poll_pause() { asm volatile("s_sleep 1" ::: "memory"); }
DEV u32 now() { return (u32)__builtin_amdgcn_s_memrealtime(); }
DEV void wait_until(u32 t) {
  _Pragma("nounroll") for (int i = 0; i < 64 && (int)(now() - t) < 0; ++i) asm volatile("s_sleep 1" ::: "memory");
}
DEV u32 umax3(u32 a, u32 b, u32 c) { u32 r; asm("v_max3_u32 %0, %1, %2, %3" : "=v"(r) : "v"(a), "v"(b), "v"(c)); return r; }
DEV u32 umax5(u32 m, u32x4 v) { return umax3(umax3(m, v.x, v.y), v.z, v.w); }
DEV float xrow_sum4(float a, float b, float c, float d) {
  auto x = __builtin_amdgcn_permlane16_swap(fbits(a), fbits(b), false, false);
  auto y = __builtin_amdgcn_permlane16_swap(fbits(c), fbits(d), false, false);
  auto z = __builtin_amdgcn_permlane32_swap(fbits(bitsf(x[0]) + bitsf(x[1])), fbits(bitsf(y[0]) + bitsf(y[1])), false, false);
  return bitsf(z[0]) + bitsf(z[1]);
}
#define RS1(i, n) "v_add_f32_dpp %" #i ", %" #i ", %" #i " row_ror:" #n " row_mask:0xf bank_mask:0xf\n\t"
#define RS16(n) RS1(0, n) RS1(1, n) RS1(2, n) RS1(3, n) RS1(4, n) RS1(5, n) RS1(6, n) RS1(7, n) RS1(8, n) RS1(9, n) RS1(10, n) RS1(11, n) RS1(12, n) RS1(13, n) RS1(14, n) RS1(15, n)
DEV void row_sum16(float (&x)[16]) {
  asm volatile("s_nop 1\n\t" RS16(8) RS16(4) RS16(2) RS16(1) "s_nop 0"
               : "+v"(x[0]), "+v"(x[1]), "+v"(x[2]), "+v"(x[3]), "+v"(x[4]), "+v"(x[5]), "+v"(x[6]), "+v"(x[7]),
                 "+v"(x[8]), "+v"(x[9]), "+v"(x[10]), "+v"(x[11]), "+v"(x[12]), "+v"(x[13]), "+v"(x[14]), "+v"(x[15]));
}
#define PL(op, d, a, so, off, m) "s_mov_b64 exec, " m "\n\t" op " " d ", " a ", %[r], " so " offen offset:" off " sc1\n\t"
#define PL_END "s_mov_b64 exec, -1\n\ts_waitcnt vmcnt(0)"
constexpr u32x4 SENT4 = {SENT, SENT, SENT, SENT};

DEV void dma4k(rsrc_t r, u32 v0, u32 v1, u32 so, u32 lds, u32 keep) {
  asm volatile(
      "s_waitcnt lgkmcnt(0)\n\t"
      "s_mov_b32 m0, %[l]\n\t"
      "s_cmp_lg_u32 %[k], 0\n\t"
      "s_cbranch_scc1 .Lkeep%=\n\t"
      "buffer_load_dwordx4 %[a], %[r], %[s] offen nt lds\n\t"
      "buffer_load_dwordx4 %[b], %[r], %[s] offen offset:1024 nt lds\n\t"
      "buffer_load_dwordx4 %[a], %[r], %[s] offen offset:2048 nt lds\n\t"
      "buffer_load_dwordx4 %[b], %[r], %[s] offen offset:3072 nt lds\n\t"
      "s_branch .Ldone%=\n"
      ".Lkeep%=:\n\t"
      "buffer_load_dwordx4 %[a], %[r], %[s] offen lds\n\t"
      "buffer_load_dwordx4 %[b], %[r], %[s] offen offset:1024 lds\n\t"
      "buffer_load_dwordx4 %[a], %[r], %[s] offen offset:2048 lds\n\t"
      "buffer_load_dwordx4 %[b], %[r], %[s] offen offset:3072 lds\n"
      ".Ldone%=:"
      :: [a] "v"(v0), [b] "v"(v1), [r] "s"(r), [s] "s"(so), [l] "s"(lds), [k] "s"(keep) : "memory", "scc");
}
DEV void dma4k_nt(rsrc_t r, u32 v0, u32 v1, u32 so, u32 lds) {
  asm volatile(
      "s_waitcnt lgkmcnt(0)\n\t"
      "s_mov_b32 m0, %[l]\n\t"
      "s_nop 0\n\t"
      "buffer_load_dwordx4 %[a], %[r], %[s] offen nt lds\n\t"
      "buffer_load_dwordx4 %[b], %[r], %[s] offen offset:1024 nt lds\n\t"
      "buffer_load_dwordx4 %[a], %[r], %[s] offen offset:2048 nt lds\n\t"
      "buffer_load_dwordx4 %[b], %[r], %[s] offen offset:3072 nt lds"
      :: [a] "v"(v0), [b] "v"(v1), [r] "s"(r), [s] "s"(so), [l] "s"(lds) : "memory");
}
DEV void dma1k(rsrc_t r, u32 voff, u32 so, u32 lds) {
  asm volatile("s_mov_b32 m0, %[l]\n\ts_nop 0\n\tbuffer_load_dwordx4 %[a], %[r], %[s] offen nt lds"
               :: [a] "v"(voff), [r] "s"(r), [s] "s"(so), [l] "s"(lds) : "memory");
}
struct Tile { bf16x8 k[8]; s16x4 v[16]; };
struct Stream { rsrc_t rK, rV; u32 dv0, dv1, slot, ka, va; };
DEV void issue_tile(const Stream& s, u32 so) {
  dma4k_nt(s.rK, s.dv0, s.dv1, so, s.slot);
  dma4k_nt(s.rK, s.dv0, s.dv1, so + 4096, s.slot + 4096);
  dma4k_nt(s.rV, s.dv0, s.dv1, so, s.slot + 8192);
  dma4k_nt(s.rV, s.dv0, s.dv1, so + 4096, s.slot + 12288);
}
template <int W, bool NT> DEV void move_tile(const Stream& s, Tile& t, u32 so, bool more, u32 keep) {
#pragma unroll
  for (int h = 0; h < 2; ++h) {
    if (more) wait_vm<W>(); else wait_vm<0>();
#pragma unroll
    for (int m = 0; m < 4; ++m) t.k[4 * h + m] = __builtin_bit_cast(bf16x8, lds128((s.ka ^ (m << 6)) + h * 4096));
    if (NT) dma4k_nt(s.rK, s.dv0, s.dv1, so + h * 4096, s.slot + h * 4096);
    else if (more) dma4k(s.rK, s.dv0, s.dv1, so + h * 4096, s.slot + h * 4096, keep);
  }
#pragma unroll
  for (int h = 0; h < 2; ++h) {
    if (more) wait_vm<W>();
#pragma unroll
    for (int m = 0; m < 8; ++m) t.v[2 * m + h] = ldstr((s.va ^ (m << 5)) + h * 4096);
    if (NT) dma4k_nt(s.rV, s.dv0, s.dv1, so + h * 4096, s.slot + 8192 + h * 4096);
    else if (more) dma4k(s.rV, s.dv0, s.dv1, so + h * 4096, s.slot + 8192 + h * 4096, keep);
  }
}

struct Soft { f32x4 acc; float m, l; };
constexpr float SCALE_LOG2E = 0.08838834764831845f * 1.4426950408889634f;
DEV void attend(const Tile& t, const bf16x8 (&q)[4], int jm, Soft& st) {
  f32x4 s0 = {0.f, 0.f, 0.f, 0.f}, s1 = {0.f, 0.f, 0.f, 0.f};
#pragma unroll
  for (int m = 0; m < 4; ++m) { s0 = mfma32(t.k[m], q[m], s0); s1 = mfma32(t.k[4 + m], q[m], s1); }
  const float mt = xrow_max(max3(max3(s0[0], s0[1], s0[2]), max3(s0[3], s1[0], s1[1]), max2(s1[2], s1[3]))) * SCALE_LOG2E;
  float p[8];
#pragma unroll
  for (int e = 0; e < 4; ++e) {
    p[e] = __builtin_amdgcn_exp2f(fmaf(s0[e], SCALE_LOG2E, -mt));
    p[4 + e] = __builtin_amdgcn_exp2f(fmaf(s1[e], SCALE_LOG2E, -mt));
  }
  const u32x4 pp = {pk2(p[0], p[1]), pk2(p[2], p[3]), pk2(p[4], p[5]), pk2(p[6], p[7])};
  f32x4 o = {0.f, 0.f, 0.f, 0.f};
#pragma unroll
  for (int m = 0; m < 8; ++m) {
    const bool on = jm == m;
    const u32x4 b = {on ? pp.x : 0u, on ? pp.y : 0u, on ? pp.z : 0u, on ? pp.w : 0u};
    o = mfma32(cat8(t.v[2 * m], t.v[2 * m + 1]), __builtin_bit_cast(bf16x8, b), o);
  }
  const float mn = max2(st.m, mt);
  const float fa = __builtin_amdgcn_exp2f(st.m - mn), fb = __builtin_amdgcn_exp2f(mt - mn);
  st.l = fmaf(st.l, fa, (((p[0] + p[1]) + (p[2] + p[3])) + ((p[4] + p[5]) + (p[6] + p[7]))) * fb);
  st.m = mn;
#pragma unroll
  for (int e = 0; e < 4; ++e) st.acc[e] = fmaf(st.acc[e], fa, o[e] * fb);
}

#define STAMP(i) do { } while (0)
#define CSTAMP(i) do { } while (0)
#define STAMP_DUMP() do { } while (0)
#define STAMP_INIT() do { } while (0)

DEV int lane_id() { int l; asm volatile("v_mbcnt_lo_u32_b32 %0, -1, 0\n\tv_mbcnt_hi_u32_b32 %0, -1, %0" : "=v"(l)); __builtin_assume(l >= 0 && l < 64); return l; }
typedef const __attribute__((address_space(4))) Params* kargs_t;
DEV kargs_t kargs() { kargs_t q = (kargs_t)__builtin_amdgcn_kernarg_segment_ptr(); asm volatile("" : "+s"(q)); return q; }
#define PHASE() \
  asm volatile("" : "+s"(wave), "+s"(b), "+s"(layer), "+s"(ws)); \
  const int lane = lane_id(), g = lane >> 4, j = lane & 15; \
  const int hk = b >> 4, sp = b & 15; \
  const bool both = sp == 7 || sp == 8; \
  const int gu = both ? 1000 : hk * 14 + (sp < 7 ? sp : sp - 2); \
  const rsrc_t rA = mk_rsrc(ws + WS_ARENA, ARENA_BYTES); \
  (void)g; (void)j; (void)hk; (void)sp; (void)both; (void)gu; (void)rA

template <int NPIECE> DEV void load_slice(const u16* w, u32 wbytes, u32 rowbytes, u32 off0, u32 lds, int wave, int lane) {
  const rsrc_t r = mk_rsrc(w, wbytes);
  const u32 vo = (lane >> 3) * rowbytes + (((lane & 7) ^ (((lane >> 4) & 3) << 1)) << 4);
#pragma unroll
  for (int i = 0; i < (NPIECE + 3) / 4; ++i)
    if (4 * i + 3 < NPIECE || 4 * i + wave < NPIECE) dma1k(r, vo, off0 + (4 * i + wave) * 8 * rowbytes, lds + (4 * i + wave) * 1024);
}
DEV void load_wq(const u16* wq, int b, bool both, int wave, int lane) {
  load_slice<18>(wq, D * HQ * DH * 2, HQ * DH * 2, (b & 3) * 144 * (HQ * DH * 2) + (b >> 2) * 128, both ? L_R2 : L_R1, wave, lane);
}
DEV void load_wo(const u16* wo, int b, int wave, int lane) {
  load_slice<32>(wo, HQ * DH * D * 2, D * 2, (b >> 4) * 256 * (D * 2) + (b & 15) * 128, L_R2, wave, lane);
}
DEV void load_gu(const u16* w, int gu, u32 lds, int wave, int lane) {
  load_slice<24>(w, D * FF * 2, FF * 2, (gu % 3) * 192 * (FF * 2) + (gu / 3) * 128, lds, wave, lane);
}
DEV void load_alias(int wave, int b) {
  asm volatile("" : "+s"(wave), "+s"(b));
  const int lane = lane_id(), hk = b >> 4, sp = b & 15;
  const int gu = hk * 14 + (sp < 7 ? sp : sp - 2);
  if (sp == 7 || sp == 8) load_wo(kargs()->wo, b, wave, lane);
  else if (gu < 216) load_gu(kargs()->wg, gu, L_R1, wave, lane);
}
DEV u32 slice_addr(u32 base, u32 row, int j) { return base + row * 128 + (((row >> 1) & 3) << 5) + (j & 3) * 8; }
DEV void ln_partial(float s, float q, int wave, int lane) {
  s += dpp<0x128>(s); q += dpp<0x128>(q); s += dpp<0x124>(s); q += dpp<0x124>(q);
  s += dpp<0x122>(s); q += dpp<0x122>(q); s += dpp<0x121>(s); q += dpp<0x121>(q);
  const float r = xrow_sum4(s, q, 0.f, 0.f);
  if ((lane & 47) == 0) sts32(L_ST + wave * 8 + (lane >> 2), fbits(r));
}
DEV void ln_stats(float& mb, float& inv) {
  const u32x4 a = lds128(L_ST), b = lds128(L_ST + 16);
  const float c = ldsf(L_ST + 32);
  const float ms = ((bitsf(a.x) + bitsf(a.z)) + (bitsf(b.x) + bitsf(b.z))) * (1.0f / D);
  const float mq = ((bitsf(a.y) + bitsf(a.w)) + (bitsf(b.y) + bitsf(b.w))) * (1.0f / D);
  const float var = fmaxf(mq - ms * ms, 0.f);
  mb = rb(c + ms);
  inv = __builtin_amdgcn_rcpf(rb(__builtin_amdgcn_sqrtf(rb(rb(var) + 1e-5f))));
}
DEV void publish_cols(rsrc_t rA, u32 rec, const f32x4 (&ac)[4], int wave, int g, int j) {
  f32x4 a;
#pragma unroll
  for (int e = 0; e < 4; ++e) {
    const float lo = (j & 1) ? ac[1][e] : ac[0][e], hi = (j & 1) ? ac[3][e] : ac[2][e];
    a[e] = (j & 2) ? hi : lo;
  }
  if (wave != 0 && j < 4) sts128(L_PP + (wave - 1) * 256 + j * 64 + g * 16, __builtin_bit_cast(u32x4, a));
  bar();
  if (wave == 0 && j < 4) {
    const u32 o = L_PP + j * 64 + g * 16;
    const f32x4 p0 = __builtin_bit_cast(f32x4, lds128(o)), p1 = __builtin_bit_cast(f32x4, lds128(o + 256)), p2 = __builtin_bit_cast(f32x4, lds128(o + 512));
    st_sc1(rA, rec + j * 64 + g * 16, canon4((a + p0) + (p1 + p2)));
  }
}
DEV void gather_cols(rsrc_t rA, int use, bool final_out, bool light, u32 first_poll, int wave, int lane) {
  const int g = lane >> 4, j = lane & 15;
  const u32 base = A_P2 + use * (144 * 256);
  const u32 p0 = 32 * wave + j, p1 = p0 + 16, p2 = 128 + 4 * wave + (j & 3);
  const u32 o0 = base + g * 2304 + wave * 512 + j * 16;
  const u32 o2 = base + (4 * g + (j >> 2)) * 2304 + 2048 + wave * 64 + (j & 3) * 16;
  const u32 pm = g == 0 ? p0 : g == 1 ? p1 : p2;
  u32x2 xr = lds64(L_XV + pm * 8);
  float c = ldsf(L_ST + 32);
  asm volatile("" : "+v"(xr), "+v"(c));
  u32x4 t[9];
  u64 m[9];
#pragma unroll
  for (int i = 0; i < 9; ++i) { t[i] = SENT4; m[i] = ~0ull; }
  wait_until(first_poll);
  if (light) {
    _Pragma("nounroll") for (u32 spin = 0; spin < SPIN_CAP; ++spin) {
      asm volatile(PL("buffer_load_dwordx4", "%0", "%[c]", "0", "0", "%[m8]") PL_END : "+v"(t[8]) : [c] "v"(o2), [r] "s"(rA), [m8] "s"(m[8]) : "memory");
      m[8] = pend4(t[8]);
      if (m[8] == 0) break;
      asm volatile("s_sleep %0" ::"i"(SLP_L) : "memory");
    }
  }
  _Pragma("nounroll") for (u32 spin = 0; spin < SPIN_CAP; ++spin) {
    asm volatile(PL("buffer_load_dwordx4", "%0", "%[a]", "0", "0", "%[m0]") PL("buffer_load_dwordx4", "%1", "%[a]", "%[s1]", "0", "%[m1]")
                 PL("buffer_load_dwordx4", "%2", "%[a]", "%[s2]", "0", "%[m2]") PL("buffer_load_dwordx4", "%3", "%[a]", "%[s3]", "0", "%[m3]")
                 PL("buffer_load_dwordx4", "%4", "%[a]", "0", "256", "%[m4]") PL("buffer_load_dwordx4", "%5", "%[a]", "%[s1]", "256", "%[m5]")
                 PL("buffer_load_dwordx4", "%6", "%[a]", "%[s2]", "256", "%[m6]") PL("buffer_load_dwordx4", "%7", "%[a]", "%[s3]", "256", "%[m7]")
                 PL("buffer_load_dwordx4", "%8", "%[c]", "0", "0", "%[m8]") PL_END
                 : "+v"(t[0]), "+v"(t[1]), "+v"(t[2]), "+v"(t[3]), "+v"(t[4]), "+v"(t[5]), "+v"(t[6]), "+v"(t[7]), "+v"(t[8])
                 : [a] "v"(o0), [c] "v"(o2), [r] "s"(rA), [s1] "s"(9216), [s2] "s"(18432), [s3] "s"(27648),
                   [m0] "s"(m[0]), [m1] "s"(m[1]), [m2] "s"(m[2]), [m3] "s"(m[3]), [m4] "s"(m[4]), [m5] "s"(m[5]), [m6] "s"(m[6]),
                   [m7] "s"(m[7]), [m8] "s"(m[8])
                 : "memory");
    const u32 mx = umax3(umax5(umax5(umax5(0, t[0]), t[1]), t[2]), umax5(umax5(umax5(0, t[3]), t[4]), t[5]), umax5(umax5(umax5(0, t[6]), t[7]), t[8]));
    if (!any_lane(mx == SENT)) break;
#pragma unroll
    for (int i = 0; i < 9; ++i) m[i] = pend4(t[i]);
    poll_pause();
  }
  float s[4];
#pragma unroll
  for (int e = 0; e < 4; ++e) {
    const float a0 = (bitsf(t[0][e]) + bitsf(t[1][e])) + (bitsf(t[2][e]) + bitsf(t[3][e]));
    const float a1 = (bitsf(t[4][e]) + bitsf(t[5][e])) + (bitsf(t[6][e]) + bitsf(t[7][e]));
    float a2 = bitsf(t[8][e]);
    a2 += dpp<0x124>(a2); a2 += dpp<0x128>(a2);
    s[e] = xrow_sum4(a0, a1, a2, a2);
  }
  const u32x2 xo = {pk2(bflo(xr.x) + rb(s[0]), bfhi(xr.x) + rb(s[1])), pk2(bflo(xr.y) + rb(s[2]), bfhi(xr.y) + rb(s[3]))};
  const bool mine = g < 2 || (g == 2 && j < 4);
  if (final_out) {
    GAS u32x2* op = (GAS u32x2*)kargs()->out;
    if (mine) op[pm] = xo;
  } else {
    if (mine) sts64(L_XV + pm * 8, xo);
    const float d0 = bflo(xo.x) - c, d1 = bfhi(xo.x) - c, d2 = bflo(xo.y) - c, d3 = bfhi(xo.y) - c;
    ln_partial(mine ? (d0 + d1) + (d2 + d3) : 0.f, mine ? fmaf(d0, d0, d1 * d1) + fmaf(d2, d2, d3 * d3) : 0.f, wave, lane);
  }
}
DEV u32 claim_issue(int n, int lim, rsrc_t rA, u32 off) {
  u32 ret;
  asm volatile("v_mov_b32 %0, 1\n\t"
               "s_cmp_lt_i32 %[n], %[lim]\n\ts_cbranch_scc1 .Lclaim_skip%=\n\t"
               "s_mov_b64 exec, 1\n\t"
               "buffer_atomic_add %0, off, %[r], %[o] sc0\n\t"
               "s_mov_b64 exec, -1\n"
               ".Lclaim_skip%=:"
               : "=&v"(ret) : [n] "s"(n), [lim] "s"(lim), [r] "s"(rA), [o] "s"(off) : "memory", "scc");
  return ret;
}

#define STREAM(PK, PV) \
  Stream sr; \
  sr.rK = mk_rsrc((PK) + (size_t)hk * SK * DH, SK * DH * 2); \
  sr.rV = mk_rsrc((PV) + (size_t)hk * SK * DH, SK * DH * 2); \
  sr.dv0 = g * 256 + ((j ^ (g << 1)) << 4); sr.dv1 = sr.dv0 ^ 128;                \
  sr.slot = L_RING + wave * 16384; \
  sr.ka = sr.slot + j * 256 + ((g ^ ((j & 7) << 1)) << 4);                        \
  sr.va = sr.slot + 8192 + (4 * g + (j >> 2)) * 256 + ((((4 * g + (j >> 2)) & 7)) << 5) + (j & 3) * 8; \
  const u32 tile0 = sp * (1u << 20) + wave * 8192
#define MOVE_TILES(T0, T1) \
  if (layer == 0) { \
    PHASE(); \
    STREAM(kargs()->k, kargs()->v); \
    _Pragma("unroll") for (int t = (T0); t < (T1); ++t) move_tile<12, true>(sr, st[t], tile0 + (t + 1) * TSTEP, true, 0); \
  }

__global__ void __launch_bounds__(256, 1) fused_decode(Params p) {
  __shared__ __attribute__((aligned(256))) unsigned char smem[163840];
  asm volatile("" ::"v"((u32)(size_t)(LAS unsigned char*)smem));
  int wave = rfl(threadIdx.x >> 6);
  int b = blockIdx.x;
  int layer = 0;
  unsigned char* ws = p.ws;
  STAMP_INIT();
  STAMP(0);

  u32 ep_lo, ep_hi;
  Tile st[NR];
  {
    PHASE();
    STREAM(p.k, p.v);
    issue_tile(sr, tile0);
    STAMP(25);
    u32x2 ep; u32x4 xa; u32 x0;
    {
      const rsrc_t rX = mk_rsrc(p.x, D * 2);
      const rsrc_t rE = mk_rsrc(ws + WS_EPOCH, 8);
      const u32 o1 = (wave * 64 + lane) * 16;
      asm volatile("buffer_load_dwordx2 %0, off, %3, 0 sc1\n\t"
                   "buffer_load_dwordx4 %1, %4, %5, 0 offen nt\n\t"
                   "buffer_load_dword %2, off, %5, 0 nt"
                   : "=&v"(ep), "=&v"(xa), "=&v"(x0) : "s"(rE), "v"(o1), "s"(rX) : "memory");
    }
    load_wq(p.wq, b, both, wave, lane);
    st_sc1(rA, b * 5120 + wave * 1024 + lane * 16, SENT4);
    if (wave == 0) st_sc1(rA, b * 5120 + 4096 + lane * 16, SENT4);
    if (sp < 7) load_wo(p.wo, b, wave, lane);
    else load_slice<36>(p.wd, FF * D * 2, D * 2, hk * 288 * (D * 2) + (sp - 7) * 128, both ? L_R1 : L_R2, wave, lane);
    if (gu < 216) load_gu(p.wu, gu, L_R1 + 24576, wave, lane);
    if (wave == 3 && lane < 24) sts128(L_ONES + lane * 16, u32x4{0x3F803F80u, 0x3F803F80u, 0x3F803F80u, 0x3F803F80u});
    asm volatile("s_waitcnt vmcnt(8)" : "+v"(ep), "+v"(xa), "+v"(x0) :: "memory");
    move_tile<63, true>(sr, st[0], tile0 + TSTEP, true, 0);
    {
      const bool mine = wave * 64 + lane < 72;
      const float c = bflo(x0);
      float sx = 0.f, sq = 0.f;
#pragma unroll
      for (int e = 0; e < 4; ++e) { const float d0 = bflo(xa[e]) - c, d1 = bfhi(xa[e]) - c; sx += d0 + d1; sq = fmaf(d0, d0, fmaf(d1, d1, sq)); }
      if (mine) sts128(L_XV + (wave * 64 + lane) * 16, xa);
      if (wave == 0 && lane == 0) sts32(L_ST + 32, fbits(c));
      ln_partial(mine ? sx : 0.f, mine ? sq : 0.f, wave, lane);
    }
    const u64 e1 = (((u64)rfl(ep.y) << 32) | rfl(ep.x)) + 1;
    ep_lo = (u32)e1; ep_hi = (u32)(e1 >> 32);
  }

  bf16x8 wf[5];
  auto load_wq_frags = [&]() {
    PHASE();
    const u32 wb = both ? L_R2 : L_R1;
    const u32 ta = slice_addr(wb, 8 * g + (j >> 2), j) ^ (wave << 5), tb = slice_addr(wb, 8 * g + 4 + (j >> 2), j) ^ (wave << 5);
#pragma unroll
    for (int i = 0; i < 4; ++i) wf[i] = cat8(ldstr(ta + i * 4096), ldstr(tb + i * 4096));
    const s16x4 w4 = ldstr(slice_addr(wb, 128 + 4 * g + (j >> 2), j) ^ (wave << 5));
    wf[4] = cat8(w4, w4);
  };
  bar();
  load_wq_frags();
  u32 tref = now() - 1000;

  for (;;) {
    f32x4 qp;
    {
      PHASE();
      STAMP(layer == 0 ? 1 : 13);
      bar();
      GAS u64* fl_ptr = (GAS u64*)(ws + WS_FLAGS) + b;
      asm volatile("" : "+s"(ep_lo), "+s"(ep_hi));
      if (layer == 0 && wave == 0 && lane == 0)
        __hip_atomic_store(fl_ptr, ((u64)ep_hi << 32) | ep_lo, __ATOMIC_RELAXED, __HIP_MEMORY_SCOPE_AGENT);
      const u32 xn = j == 1 ? L_ONES : L_XV + (b & 3) * 288;
      bf16x8 xf[4];
#pragma unroll
      for (int i = 0; i < 4; ++i) xf[i] = __builtin_bit_cast(bf16x8, lds128(xn + i * 64 + g * 16));
      const u32x2 x4 = lds64(xn + 256 + g * 8);
      float mb, inv;
      ln_stats(mb, inv);
      f32x4 aq = mfma16(__builtin_bit_cast(s16x4, __builtin_bit_cast(u32x4, wf[4]).xy), __builtin_bit_cast(s16x4, x4), f32x4{0.f, 0.f, 0.f, 0.f});
#pragma unroll
      for (int i = 0; i < 4; ++i) aq = mfma32(wf[i], xf[i], aq);
#pragma unroll
      for (int e = 0; e < 4; ++e) qp[e] = (aq[e] - mb * dpp<0xB1>(aq[e])) * inv;
    }
    MOVE_TILES(1, (NR + 1) / 2);
    {
      PHASE();
      if (layer == 0) {
        const rsrc_t rF = mk_rsrc(ws + WS_FLAGS, NWG * 8);
        _Pragma("nounroll") for (u32 spin = 0; spin < SPIN_CAP; ++spin) {
          const u32x4 f0 = ld_sc1(rF, lane * 32), f1 = ld_sc1(rF, lane * 32 + 16);
          const u32 bad = ((f0.x ^ ep_lo) | (f0.y ^ ep_hi)) | ((f0.z ^ ep_lo) | (f0.w ^ ep_hi)) | ((f1.x ^ ep_lo) | (f1.y ^ ep_hi)) | ((f1.z ^ ep_lo) | (f1.w ^ ep_hi));
          if (!any_lane(bad != 0)) break;
          poll_pause();
        }
      }
      if (j == 0) st_sc1(rA, A_Q + (layer * 256 + b) * 256 + wave * 64 + g * 16, canon4(qp));
      STAMP(layer == 0 ? 2 : 14);
    }
    MOVE_TILES((NR + 1) / 2, NR);

    Soft sm;
    {
      PHASE();
      STREAM(kargs()->k, kargs()->v);
      const int jm = j >> 1;
      STAMP(layer == 0 ? 3 : 15);
      bf16x8 q[4];
      {
        const u32 off = A_Q + (layer * 256 + (4 * hk + wave) * 4 + g) * 256 + j * 16;
        u32x4 t = SENT4;
        u64 m0 = ~0ull;
        wait_until(tref + DLY_Q);
        _Pragma("nounroll") for (u32 spin = 0; spin < SPIN_CAP; ++spin) {
          asm volatile(PL("buffer_load_dwordx4", "%0", "%[a]", "0", "0", "%[m0]") PL_END : "+v"(t) : [a] "v"(off), [r] "s"(rA), [m0] "s"(m0) : "memory");
          m0 = pend4(t);
          if (m0 == 0) break;
          poll_pause();
        }
        const float qv = xrow_sum4(bitsf(t.x), bitsf(t.y), bitsf(t.z), bitsf(t.w));
        *(LAS u16*)(size_t)(L_QB + wave * 128 + j * 8 + g * 2) = (u16)pk2(qv, qv);
        bar();
#pragma unroll
        for (int m = 0; m < 4; ++m) q[m] = __builtin_bit_cast(bf16x8, lds128(L_QB + (j & 1) * 256 + m * 64 + g * 16));
      }
      STAMP(layer == 0 ? 4 : 16);
      if (layer == 1) CSTAMP(28);

      sm.acc = f32x4{0.f, 0.f, 0.f, 0.f}; sm.m = -INFINITY; sm.l = 0.f;
      const int NS = layer == 0 ? NSTEP - 1 : NS1;
      constexpr u32 NPOOL = 16 * (NSTEP - 1 - NL2 - NS1);
      int stepn = layer == 0 ? NR + 1 : NR - NL2;
      u32 nx = sp * 32 + (stepn < NR ? stepn + NSTEP - 1 - NR : stepn);
      u32 fin = layer == 0 ? sp * 32 + NSTEP - 1 : NONE;
      int lim = wave == 0 && layer == 1 ? NS - 1 : 1000;
      constexpr u32 L2BITS = ((1u << NL2) - 1u) << (NSTEP - 1 - NL2);
      const u32 kgate = layer == 0 ? 0x7FFFFFFFu : 0u;
      const u32 cl_off = A_CLAIM + (layer * 16 + hk) * 128;
      const int sphase = b & 1;
      _Pragma("nounroll") for (int it = 0;; ++it) {
        const bool more = nx != NONE;
        u32 claimv = claim_issue(stepn, lim, rA, cl_off);
        Tile tl;
        const u32 keep = ((KEEPMASK >> ((nx + (nx >> 5) * KEEPMUL + hk) & 31)) | (L2BITS >> (nx & 31))) & (kgate >> (nx & 31)) & 1;
        move_tile<12, false>(sr, tl, nx * TSTEP + wave * 8192, more, keep);
        if (wave == 0) {
          u32 ny;
          if (stepn + 1 < NS) { ++stepn; ny = sp * 32 + (stepn < NR ? stepn + NSTEP - 1 - NR : stepn); }
          else {
            stepn = NS;
            asm volatile("" : "+v"(claimv) :: "memory");
            const u32 c = rfl(claimv) + 1;
            if (lim != 1000 && c < NPOOL) ny = (c & 15) * 32 + NS + (c >> 4);
            else { lim = 1000; ny = fin; fin = NONE; }
          }
          sts32(L_NX + (it & 1) * 4, ny);
        }
        if (it == 0) load_alias(wave, b);
        attend(tl, q, jm, sm);
#pragma unroll
        for (int t = 0; t < NR; ++t)
          if (it == 2 * t + sphase) attend(st[t], q, jm, sm);
        if (!more) break;
        bar();
        nx = rfl(lds32(L_NX + (it & 1) * 4));
      }
      STAMP(layer == 0 ? 5 : 17);
      if (layer == 0) CSTAMP(27); else CSTAMP(29);
    }
    {
      PHASE();
      const float lt = xrow_sum(sm.l);
      if (wave != 0) sts128(L_CMB + (wave - 1) * 1024 + lane * 16, __builtin_bit_cast(u32x4, sm.acc));
      if (lane < 2) sts64(L_CML + wave * 16 + lane * 8, u32x2{fbits(sm.m), fbits(lt)});
      bar();
      if (wave == 0) {
        float mw[4], lw[4];
#pragma unroll
        for (int w = 0; w < 4; ++w) { const u32x2 t = lds64(L_CML + w * 16 + (j & 1) * 8); mw[w] = bitsf(t.x); lw[w] = bitsf(t.y); }
        const float M = max2(max2(mw[0], mw[1]), max2(mw[2], mw[3]));
        float sc[4], L = 0.f;
#pragma unroll
        for (int w = 0; w < 4; ++w) { sc[w] = __builtin_amdgcn_exp2f(mw[w] - M); L = fmaf(sc[w], lw[w], L); }
        f32x4 o = sm.acc * sc[0];
#pragma unroll
        for (int w = 1; w < 4; ++w) o += __builtin_bit_cast(f32x4, lds128(L_CMB + (w - 1) * 1024 + lane * 16)) * sc[w];
        const u32 rec = A_P1 + (layer * 256 + b) * 1152;
        st_sc1(rA, rec + (j & 1) * 512 + (j >> 1) * 64 + g * 16, canon4(o));
        if (lane < 2) st_sc1_64(rA, rec + 1024 + lane * 8, u32x2{canon(fbits(M)), canon(fbits(L))});
      }
      STAMP(layer == 0 ? 6 : 18);
    }
    {
      PHASE();
      if (sp < 9) {
        bf16x8 af[8];
        const u32 ta = slice_addr(L_R2, 64 * wave + 4 * g + (j >> 2), j);
#pragma unroll
        for (int i = 0; i < 8; ++i) af[i] = cat8(ldstr((ta ^ ((i & 3) << 5)) + (i >> 2) * 4096), ldstr((ta ^ ((i & 3) << 5)) + (i >> 2) * 4096 + 2048));
        u32x4 t[4] = {SENT4, SENT4, SENT4, SENT4}; u32x2 ml = {SENT, SENT};
        const u32 rec = A_P1 + (layer * 256 + 16 * hk + j) * 1152;
        const u32 o1 = rec + wave * 256 + g * 16, o2 = rec + 1024 + (wave >> 1) * 8;
        u64 m0 = H1LIGHT ? 0ull : ~0ull, m1 = ~0ull;
        _Pragma("nounroll") for (u32 spin = 0; spin < SPIN_CAP; ++spin) {
          asm volatile("s_mov_b64 exec, %[m1]\n\t"
                       "buffer_load_dwordx2 %4, %[b], %[r], 0 offen sc1\n\t"
                       "s_mov_b64 exec, %[m0]\n\t"
                       "buffer_load_dwordx4 %0, %[a], %[r], 0 offen sc1\n\t"
                       "buffer_load_dwordx4 %1, %[a], %[r], 0 offen offset:64 sc1\n\t"
                       "buffer_load_dwordx4 %2, %[a], %[r], 0 offen offset:128 sc1\n\t"
                       "buffer_load_dwordx4 %3, %[a], %[r], 0 offen offset:192 sc1\n\t" PL_END
                       : "+v"(t[0]), "+v"(t[1]), "+v"(t[2]), "+v"(t[3]), "+v"(ml)
                       : [a] "v"(o1), [b] "v"(o2), [r] "s"(rA), [m0] "s"(m0), [m1] "s"(m1) : "memory");
          m1 = __builtin_amdgcn_ballot_w64(umax(ml.x, ml.y) == SENT);
          m0 = __builtin_amdgcn_ballot_w64(umax(umax5(umax5(0, t[0]), t[1]), umax5(umax5(0, t[2]), t[3])) == SENT);
          if ((m0 | m1) == 0) break;
          if (H1LIGHT) m0 &= ~m1;
          if (m0 == 0 || !H1LIGHT) asm volatile("s_sleep %0" ::"i"(SLP_L) : "memory");
        }
        STAMP(layer == 0 ? 7 : 19);
        float M = bitsf(ml.x);
        M = max2(M, dpp<0x128>(M)); M = max2(M, dpp<0x124>(M)); M = max2(M, dpp<0x122>(M)); M = max2(M, dpp<0x121>(M));
        const float w0 = __builtin_amdgcn_exp2f(bitsf(ml.x) - M);
        float ov[16];
#pragma unroll
        for (int i = 0; i < 16; ++i) ov[i] = w0 * bitsf(t[i >> 2][i & 3]);
        const float inv = __builtin_amdgcn_rcpf(row_sum(w0 * bitsf(ml.y)));
        row_sum16(ov);
        f32x4 ac[4];
#pragma unroll
        for (int kk = 0; kk < 2; ++kk) {
          const u32x4 ob = {pk2(ov[8 * kk] * inv, ov[8 * kk + 1] * inv), pk2(ov[8 * kk + 2] * inv, ov[8 * kk + 3] * inv),
                            pk2(ov[8 * kk + 4] * inv, ov[8 * kk + 5] * inv), pk2(ov[8 * kk + 6] * inv, ov[8 * kk + 7] * inv)};
#pragma unroll
          for (int c = 0; c < 4; ++c) ac[c] = mfma32(af[4 * kk + c], __builtin_bit_cast(bf16x8, ob), kk ? ac[c] : f32x4{0.f, 0.f, 0.f, 0.f});
        }
        publish_cols(rA, A_P2 + ((2 * layer) * 144 + hk * 9 + sp) * 256, ac, wave, g, j);
        STAMP(layer == 0 ? 8 : 20);
      }
    }
    {
      PHASE();
      bf16x8 gf[12];
      {
        const u32 ta = slice_addr(L_R1, 8 * g + (j >> 2), j) ^ (wave << 5), tb = slice_addr(L_R1, 8 * g + 4 + (j >> 2), j) ^ (wave << 5);
#pragma unroll
        for (int i = 0; i < 12; ++i) gf[i] = cat8(ldstr(ta + i * 4096), ldstr(tb + i * 4096));
      }
      gather_cols(rA, 2 * layer, false, true, tref, wave, lane);
      STAMP(layer == 0 ? 9 : 21);
      tref = now();
      bar();
      if (layer == 0) load_wq(kargs()->wq, b, both, wave, lane);
      if (gu < 216) {
        const u32 xn = (j == 1 ? L_ONES : L_XV + (gu % 3) * 384) + g * 16;
        bf16x8 xf[6];
#pragma unroll
        for (int i = 0; i < 6; ++i) xf[i] = __builtin_bit_cast(bf16x8, lds128(xn + i * 64));
        float mb, inv;
        ln_stats(mb, inv);
        f32x4 ag = {0.f, 0.f, 0.f, 0.f}, au = ag;
#pragma unroll
        for (int i = 0; i < 6; ++i) { ag = mfma32(gf[i], xf[i], ag); au = mfma32(gf[6 + i], xf[i], au); }
        f32x4 r;
#pragma unroll
        for (int e = 0; e < 4; ++e) {
          const float sg = dpp<0xB1>(ag[e]), su = dpp<0xB1>(au[e]);
          r[e] = ((j == 0 ? ag[e] : su) - mb * (j == 0 ? sg : au[e])) * inv;
        }
        if (j < 2) st_sc1(rA, A_G + (layer * 216 + gu) * 512 + wave * 128 + j * 64 + g * 16, canon4(r));
      }
      STAMP(layer == 0 ? 10 : 22);
    }
    {
      PHASE();
      if (sp >= 7) {
        bf16x8 af[12];
        const u32 wb = both ? L_R1 : L_R2;
        const u32 ta = slice_addr(wb, 64 * wave + 8 * g + (j >> 2), j), tb = slice_addr(wb, 64 * wave + 8 * g + 4 + (j >> 2), j);
#pragma unroll
        for (int i = 0; i < 12; ++i) {
          const u32 ko = i < 8 || wave == 3 ? (i >> 2) * 4096 : 0;
          af[i] = cat8(ldstr((ta ^ ((i & 3) << 5)) + ko), ldstr((tb ^ ((i & 3) << 5)) + ko));
        }
        u32x4 t[4] = {SENT4, SENT4, SENT4, SENT4};
        const u32 u1 = 288 * hk + 64 * wave + 4 * j, u2 = 288 * hk + 256 + 4 * (j & 7);
        const u32 gb = A_G + (layer * 216 + g) * 512;
        const u32 o1 = g < 3 ? gb + (u1 >> 6) * 1536 + ((u1 >> 4) & 3) * 128 + (u1 & 15) * 4 : OOB;
        const u32 o2 = g < 3 && wave == 3 ? gb + (u2 >> 6) * 1536 + ((u2 >> 4) & 3) * 128 + (u2 & 15) * 4 : OOB;
        u64 m0 = ~0ull;
        wait_until(tref + DLY_H);
        _Pragma("nounroll") for (u32 spin = 0; spin < SPIN_CAP; ++spin) {
          asm volatile("s_mov_b64 exec, %[m0]\n\t"
                       "buffer_load_dwordx4 %0, %[a], %[r], 0 offen sc1\n\t"
                       "buffer_load_dwordx4 %1, %[a], %[r], 0 offen offset:64 sc1\n\t"
                       "buffer_load_dwordx4 %2, %[b], %[r], 0 offen sc1\n\t"
                       "buffer_load_dwordx4 %3, %[b], %[r], 0 offen offset:64 sc1\n\t" PL_END
                       : "+v"(t[0]), "+v"(t[1]), "+v"(t[2]), "+v"(t[3])
                       : [a] "v"(o1), [b] "v"(o2), [r] "s"(rA), [m0] "s"(m0) : "memory");
          m0 = __builtin_amdgcn_ballot_w64(umax(umax5(umax5(0, t[0]), t[1]), umax5(umax5(0, t[2]), t[3])) == SENT);
          if (m0 == 0) break;
          poll_pause();
        }
        STAMP(layer == 0 ? 11 : 23);
        auto hval = [&](u32x4 tg, u32x4 tu) {
          const float gv = rb(xrow_sum4(bitsf(tg.x), bitsf(tg.y), bitsf(tg.z), bitsf(tg.w)));
          const float uv = rb(xrow_sum4(bitsf(tu.x), bitsf(tu.y), bitsf(tu.z), bitsf(tu.w)));
          const float h = (gv * (gv > 0.f ? 1.f : 0.f)) * uv;
          return (u16)pk2(h, h);
        };
        const u16 h1 = hval(t[0], t[1]), h2 = hval(t[2], t[3]);
        *(LAS u16*)(size_t)(L_HB + wave * 192 + j * 8 + g * 2) = h1;
        if (j < 8) *(LAS u16*)(size_t)(L_HB + wave * 192 + 128 + j * 8 + g * 2) = h2;
        f32x4 ac[4];
#pragma unroll
        for (int kk = 0; kk < 3; ++kk) {
          const bf16x8 hb = __builtin_bit_cast(bf16x8, lds128(L_HB + wave * 192 + kk * 64 + g * 16));
#pragma unroll
          for (int c = 0; c < 4; ++c) ac[c] = mfma32(af[4 * kk + c], hb, kk ? ac[c] : f32x4{0.f, 0.f, 0.f, 0.f});
        }
        publish_cols(rA, A_P2 + ((2 * layer + 1) * 144 + hk * 9 + sp - 7) * 256, ac, wave, g, j);
        STAMP(layer == 0 ? 12 : 24);
      } else {
        wait_vm<0>();
        bar();
      }
    }
    load_wq_frags();
    {
      PHASE();
      if (layer == 0 || b == 0) gather_cols(rA, 2 * layer + 1, layer == 1, X2LIGHT, tref + DLY_X2, wave, lane);
      if (layer == 1) {
        GAS u64* ep_ptr = (GAS u64*)(ws + WS_EPOCH);
        asm volatile("" : "+s"(ep_lo), "+s"(ep_hi));
        if (wave == 0 && b == 0 && lane == 0)
          __hip_atomic_store(ep_ptr, ((u64)ep_hi << 32) | ep_lo, __ATOMIC_RELAXED, __HIP_MEMORY_SCOPE_AGENT);
        STAMP(26);
        STAMP_DUMP();
        break;
      }
      tref = now();
      layer = 1;
    }
  }
}

extern "C" void kernel_launch(void* const* d_in, const int* in_sizes, int n_in, void* d_out, int out_size, void* d_ws,
                              size_t ws_size, hipStream_t stream) {
  Params p;
  p.x = (const u16*)d_in[0]; p.k = (const u16*)d_in[1]; p.v = (const u16*)d_in[2];
  p.wq = (const u16*)d_in[3]; p.wo = (const u16*)d_in[4];
  p.wg = (const u16*)d_in[6]; p.wu = (const u16*)d_in[7]; p.wd = (const u16*)d_in[8];
  p.out = (u16*)d_out; p.ws = (unsigned char*)d_ws;
  hipLaunchKernelGGL(fused_decode, dim3(NWG), dim3(256), 0, stream, p);
}
```

```cpp
#include <hip/hip_runtime.h>
#include <hip/hip_bf16.h>

typedef unsigned short u16;
typedef unsigned int u32;
typedef unsigned long long u64;
typedef __bf16 bf16x8 __attribute__((ext_vector_type(8)));
typedef __bf16 bf16x2 __attribute__((ext_vector_type(2)));
typedef short s16x4 __attribute__((ext_vector_type(4)));
typedef float f32x4 __attribute__((ext_vector_type(4)));
typedef float f32x2 __attribute__((ext_vector_type(2)));
typedef u32 u32x4 __attribute__((ext_vector_type(4)));
typedef u32 u32x2 __attribute__((ext_vector_type(2)));
typedef __amdgpu_buffer_rsrc_t rsrc_t;
#define DEV __device__ __forceinline__
#define LAS __attribute__((address_space(3)))
#define GAS __attribute__((address_space(1)))

#define NR 6
#define ABL 0
#define KEEPMASK 0x49249249u
#define NS0 (NSTEP - 1 - NL2)
#define KEEPALL 0u
#define KEEPLIM 0x7FFFFFFFu
#define L2POS (1 + ((b >> 3) & 7))
#define NL2 2
#define NS1 23
#define KEEPMUL 1
#define STAMPS 0
#define SLP_L 3
#define H1LIGHT 0
#define DLY_X1 25
#define DLY_X1N 150
#define SLP_X1 2
#define DLY_H 100
#define DLY_X2 240
#define DLY_Q 80

constexpr int D = 576, HQ = 32, HKV = 16, SK = 65536, DH = 128, FF = 4608;
constexpr int NWG = 256;
constexpr int NSTEP = 32;
constexpr u32 TSTEP = 32768;
constexpr u32 SENT = 0xFFFFFFFFu;
constexpr u32 OOB = 0x7FFFFF00u;
constexpr u32 SPIN_CAP = 1u << 18;
constexpr u32 NONE = 0xFFFFFFFFu;
static_assert(2 * NR + 1 <= NS1 - NR && NS1 <= NSTEP - 1 - NL2 && 2 * NR <= NS0 - NR && NS0 <= NSTEP - 1 - NL2, "resident tiles are spread over the static iterations");

constexpr u32 WS_EPOCH = 0, WS_FLAGS = 4096, WS_ARENA = 8192;
constexpr u32 A_CLAIM = 0;
constexpr u32 A_Q = 36864;
constexpr u32 A_G = A_Q + 2 * 256 * 256;
constexpr u32 A_P1 = A_G + 2 * 216 * 512;
constexpr u32 A_P2 = A_P1 + 2 * 256 * 1152;
constexpr u32 A_END = A_P2 + 4 * 144 * 256;
constexpr u32 ARENA_BYTES = NWG * 5120;
static_assert(A_END <= ARENA_BYTES, "arena");
constexpr u32 WS_STAMP = WS_ARENA + ARENA_BYTES;
constexpr u32 WS_BYTES = WS_STAMP + NWG * 4 * 32 * 8;

constexpr u32 L_RING = 0;
constexpr u32 L_R1 = 65536;
constexpr u32 L_R2 = L_R1 + 49152;
constexpr u32 L_XV = L_R2 + 36864;
constexpr u32 L_ONES = L_XV + 1152;
constexpr u32 L_CMB = L_ONES + 384;
constexpr u32 L_CML = L_CMB + 3072;
constexpr u32 L_PP = L_CML + 64;
constexpr u32 L_HB = L_PP + 768;
constexpr u32 L_QB = L_HB + 768;
constexpr u32 L_ST = L_QB + 512;
constexpr u32 L_NX = L_ST + 48;
constexpr u32 L_END = L_NX + 16;
static_assert(L_END + 512 <= 163840, "lds");

struct Params {
  const u16* x; const u16* k; const u16* v; const u16* wq; const u16* wo;
  const u16* wg; const u16* wu; const u16* wd; u16* out; unsigned char* ws;
};

DEV u32 rfl(u32 x) { return __builtin_amdgcn_readfirstlane(x); }
DEV rsrc_t mk_rsrc(const void* p, u32 bytes) { return __builtin_amdgcn_make_buffer_rsrc((void*)p, 0, bytes, 0x00020000); }
DEV u32x4 lds128(u32 a) { return *(const LAS u32x4*)(size_t)a; }
DEV u32x2 lds64(u32 a) { return *(const LAS u32x2*)(size_t)a; }
DEV u32 lds32(u32 a) { return *(const LAS u32*)(size_t)a; }
DEV float ldsf(u32 a) { return *(const LAS float*)(size_t)a; }
DEV void sts128(u32 a, u32x4 v) { *(LAS u32x4*)(size_t)a = v; }
DEV void sts64(u32 a, u32x2 v) { *(LAS u32x2*)(size_t)a = v; }
DEV void sts32(u32 a, u32 v) { *(LAS u32*)(size_t)a = v; }
DEV s16x4 ldstr(u32 a) { return __builtin_amdgcn_ds_read_tr16_b64_v4i16((LAS s16x4*)(size_t)a); }
DEV bf16x8 cat8(s16x4 lo, s16x4 hi) {
  typedef short s16x8 __attribute__((ext_vector_type(8)));
  s16x8 r = __builtin_shufflevector(lo, hi, 0, 1, 2, 3, 4, 5, 6, 7);
  return __builtin_bit_cast(bf16x8, r);
}
DEV f32x4 mfma32(bf16x8 a, bf16x8 b, f32x4 c) { return __builtin_amdgcn_mfma_f32_16x16x32_bf16(a, b, c, 0, 0, 0); }
DEV f32x4 mfma16(s16x4 a, s16x4 b, f32x4 c) { return __builtin_amdgcn_mfma_f32_16x16x16bf16_1k(a, b, c, 0, 0, 0); }
DEV u32 pk2(float lo, float hi) { f32x2 v = {lo, hi}; return __builtin_bit_cast(u32, __builtin_convertvector(v, bf16x2)); }
DEV float bflo(u32 w) { return __builtin_bit_cast(float, w << 16); }
DEV float bfhi(u32 w) { return __builtin_bit_cast(float, w & 0xffff0000u); }
DEV float rb(float f) { return bflo(pk2(f, f)); }
DEV u32 canon(u32 w) { return w == SENT ? 0x7FFFFFFFu : w; }
DEV u32 fbits(float f) { return __builtin_bit_cast(u32, f); }
DEV float bitsf(u32 u) { return __builtin_bit_cast(float, u); }
DEV u32x4 canon4(f32x4 a) { return u32x4{canon(fbits(a[0])), canon(fbits(a[1])), canon(fbits(a[2])), canon(fbits(a[3]))}; }
DEV float max2(float a, float b) { float r; asm("v_max_f32_e32 %0, %1, %2" : "=v"(r) : "v"(a), "v"(b)); return r; }
DEV float max3(float a, float b, float c) { float r; asm("v_max3_f32 %0, %1, %2, %3" : "=v"(r) : "v"(a), "v"(b), "v"(c)); return r; }
template <int CTRL> DEV float dpp(float x) { return bitsf(__builtin_amdgcn_mov_dpp(fbits(x), CTRL, 0xf, 0xf, true)); }
DEV float row_sum(float x) { x += dpp<0x128>(x); x += dpp<0x124>(x); x += dpp<0x122>(x); x += dpp<0x121>(x); return x; }
DEV float row_max(float x) { x = fmaxf(x, dpp<0x128>(x)); x = fmaxf(x, dpp<0x124>(x)); x = fmaxf(x, dpp<0x122>(x)); x = fmaxf(x, dpp<0x121>(x)); return x; }
DEV float xrow_sum(float x) {
  auto s = __builtin_amdgcn_permlane16_swap(fbits(x), fbits(x), false, false);
  x = bitsf(s[0]) + bitsf(s[1]);
  auto t = __builtin_amdgcn_permlane32_swap(fbits(x), fbits(x), false, false);
  return bitsf(t[0]) + bitsf(t[1]);
}
DEV float xrow_max(float x) {
  auto s = __builtin_amdgcn_permlane16_swap(fbits(x), fbits(x), false, false);
  x = max2(bitsf(s[0]), bitsf(s[1]));
  auto t = __builtin_amdgcn_permlane32_swap(fbits(x), fbits(x), false, false);
  return max2(bitsf(t[0]), bitsf(t[1]));
}
DEV float wave_sum(float x) { return xrow_sum(row_sum(x)); }
DEV void bar() { asm volatile("s_waitcnt lgkmcnt(0)\n\ts_barrier" ::: "memory"); }
template <int N> DEV void wait_vm() { asm volatile("s_waitcnt vmcnt(%0)" ::"i"(N) : "memory"); }
DEV u32 umax(u32 a, u32 b) { return a > b ? a : b; }
DEV u32 umax4(u32x4 v) { return umax(umax(v.x, v.y), umax(v.z, v.w)); }
DEV u64 pend4(u32x4 v) { return __builtin_amdgcn_ballot_w64(umax4(v) == SENT); }
DEV bool any_lane(bool b) { return __builtin_amdgcn_ballot_w64(b) != 0; }
DEV u32x4 ld_sc1(rsrc_t r, u32 off) { return __builtin_amdgcn_raw_buffer_load_b128(r, off, 0, 16); }
DEV void st_sc1(rsrc_t r, u32 off, u32x4 v) { __builtin_amdgcn_raw_buffer_store_b128(v, r, off, 0, 16); }
DEV void st_sc1_64(rsrc_t r, u32 off, u32x2 v) { __builtin_amdgcn_raw_buffer_store_b64(v, r, off, 0, 16); }
DEV void st_sc1_32(rsrc_t r, u32 off, u32 v) { __builtin_amdgcn_raw_buffer_store_b32(v, r, off, 0, 16); }
DEV void poll_pause() { asm volatile("s_sleep 1" ::: "memory"); }
DEV u32 now() { return (u32)__builtin_amdgcn_s_memrealtime(); }
DEV void wait_until(u32 t) {
  _Pragma("nounroll") for (int i = 0; i < 64 && (int)(now() - t) < 0; ++i) asm volatile("s_sleep 1" ::: "memory");
}
DEV u32 umax3(u32 a, u32 b, u32 c) { u32 r; asm("v_max3_u32 %0, %1, %2, %3" : "=v"(r) : "v"(a), "v"(b), "v"(c)); return r; }
DEV u32 umax5(u32 m, u32x4 v) { return umax3(umax3(m, v.x, v.y), v.z, v.w); }
DEV float xrow_sum4(float a, float b, float c, float d) {
  auto x = __builtin_amdgcn_permlane16_swap(fbits(a), fbits(b), false, false);
  auto y = __builtin_amdgcn_permlane16_swap(fbits(c), fbits(d), false, false);
  auto z = __builtin_amdgcn_permlane32_swap(fbits(bitsf(x[0]) + bitsf(x[1])), fbits(bitsf(y[0]) + bitsf(y[1])), false, false);
  return bitsf(z[0]) + bitsf(z[1]);
}
#define RS1(i, n) "v_add_f32_dpp %" #i ", %" #i ", %" #i " row_ror:" #n " row_mask:0xf bank_mask:0xf\n\t"
#define RS16(n) RS1(0, n) RS1(1, n) RS1(2, n) RS1(3, n) RS1(4, n) RS1(5, n) RS1(6, n) RS1(7, n) RS1(8, n) RS1(9, n) RS1(10, n) RS1(11, n) RS1(12, n) RS1(13, n) RS1(14, n) RS1(15, n)
DEV void row_sum16(float (&x)[16]) {
  asm volatile("s_nop 1\n\t" RS16(8) RS16(4) RS16(2) RS16(1) "s_nop 0"
               : "+v"(x[0]), "+v"(x[1]), "+v"(x[2]), "+v"(x[3]), "+v"(x[4]), "+v"(x[5]), "+v"(x[6]), "+v"(x[7]),
                 "+v"(x[8]), "+v"(x[9]), "+v"(x[10]), "+v"(x[11]), "+v"(x[12]), "+v"(x[13]), "+v"(x[14]), "+v"(x[15]));
}
#define PL(op, d, a, so, off, m) "s_mov_b64 exec, " m "\n\t" op " " d ", " a ", %[r], " so " offen offset:" off " sc1\n\t"
#define PL_END "s_mov_b64 exec, -1\n\ts_waitcnt vmcnt(0)"
constexpr u32x4 SENT4 = {SENT, SENT, SENT, SENT};

DEV void dma4k(rsrc_t r, u32 v0, u32 v1, u32 so, u32 lds, u32 keep) {
  asm volatile(
      "s_waitcnt lgkmcnt(0)\n\t"
      "s_mov_b32 m0, %[l]\n\t"
      "s_cmp_lg_u32 %[k], 0\n\t"
      "s_cbranch_scc1 .Lkeep%=\n\t"
      "buffer_load_dwordx4 %[a], %[r], %[s] offen nt lds\n\t"
      "buffer_load_dwordx4 %[b], %[r], %[s] offen offset:1024 nt lds\n\t"
      "buffer_load_dwordx4 %[a], %[r], %[s] offen offset:2048 nt lds\n\t"
      "buffer_load_dwordx4 %[b], %[r], %[s] offen offset:3072 nt lds\n\t"
      "s_branch .Ldone%=\n"
      ".Lkeep%=:\n\t"
      "buffer_load_dwordx4 %[a], %[r], %[s] offen lds\n\t"
      "buffer_load_dwordx4 %[b], %[r], %[s] offen offset:1024 lds\n\t"
      "buffer_load_dwordx4 %[a], %[r], %[s] offen offset:2048 lds\n\t"
      "buffer_load_dwordx4 %[b], %[r], %[s] offen offset:3072 lds\n"
      ".Ldone%=:"
      :: [a] "v"(v0), [b] "v"(v1), [r] "s"(r), [s] "s"(so), [l] "s"(lds), [k] "s"(keep) : "memory", "scc");
}
DEV void dma4k_nt(rsrc_t r, u32 v0, u32 v1, u32 so, u32 lds) {
  asm volatile(
      "s_waitcnt lgkmcnt(0)\n\t"
      "s_mov_b32 m0, %[l]\n\t"
      "s_nop 0\n\t"
      "buffer_load_dwordx4 %[a], %[r], %[s] offen nt lds\n\t"
      "buffer_load_dwordx4 %[b], %[r], %[s] offen offset:1024 nt lds\n\t"
      "buffer_load_dwordx4 %[a], %[r], %[s] offen offset:2048 nt lds\n\t"
      "buffer_load_dwordx4 %[b], %[r], %[s] offen offset:3072 nt lds"
      :: [a] "v"(v0), [b] "v"(v1), [r] "s"(r), [s] "s"(so), [l] "s"(lds) : "memory");
}
DEV void dma1k(rsrc_t r, u32 voff, u32 so, u32 lds) {
  asm volatile("s_mov_b32 m0, %[l]\n\ts_nop 0\n\tbuffer_load_dwordx4 %[a], %[r], %[s] offen nt lds"
               :: [a] "v"(voff), [r] "s"(r), [s] "s"(so), [l] "s"(lds) : "memory");
}
struct Tile { bf16x8 k[8]; s16x4 v[16]; };
struct Stream { rsrc_t rK, rV; u32 dv0, dv1, slot, ka, va; };
DEV void issue_tile(const Stream& s, u32 so, u32) {
  dma4k_nt(s.rK, s.dv0, s.dv1, so, s.slot);
  dma4k_nt(s.rK, s.dv0, s.dv1, so + 4096, s.slot + 4096);
  dma4k_nt(s.rV, s.dv0, s.dv1, so, s.slot + 8192);
  dma4k_nt(s.rV, s.dv0, s.dv1, so + 4096, s.slot + 12288);
}
template <int W, bool NT> DEV void move_tile(const Stream& s, Tile& t, u32 so, bool more, u32 keep) {
#pragma unroll
  for (int h = 0; h < 2; ++h) {
    if (more) wait_vm<W>(); else wait_vm<0>();
#pragma unroll
    for (int m = 0; m < 4; ++m) t.k[4 * h + m] = __builtin_bit_cast(bf16x8, lds128((s.ka ^ (m << 6)) + h * 4096));
    if (NT) dma4k_nt(s.rK, s.dv0, s.dv1, so + h * 4096, s.slot + h * 4096);
    else if (more) dma4k(s.rK, s.dv0, s.dv1, so + h * 4096, s.slot + h * 4096, keep);
  }
#pragma unroll
  for (int h = 0; h < 2; ++h) {
    if (more) wait_vm<W>();
#pragma unroll
    for (int m = 0; m < 8; ++m) t.v[2 * m + h] = ldstr((s.va ^ (m << 5)) + h * 4096);
    if (NT) dma4k_nt(s.rV, s.dv0, s.dv1, so + h * 4096, s.slot + 8192 + h * 4096);
    else if (more) dma4k(s.rV, s.dv0, s.dv1, so + h * 4096, s.slot + 8192 + h * 4096, keep);
  }
}

struct Soft { f32x4 acc; float m, l; };
constexpr float SCALE_LOG2E = 0.08838834764831845f * 1.4426950408889634f;
DEV void attend(const Tile& t, const bf16x8 (&q)[4], int jm, Soft& st) {
  f32x4 s0 = {0.f, 0.f, 0.f, 0.f}, s1 = {0.f, 0.f, 0.f, 0.f};
#pragma unroll
  for (int m = 0; m < 4; ++m) { s0 = mfma32(t.k[m], q[m], s0); s1 = mfma32(t.k[4 + m], q[m], s1); }
  const float mt = xrow_max(max3(max3(s0[0], s0[1], s0[2]), max3(s0[3], s1[0], s1[1]), max2(s1[2], s1[3]))) * SCALE_LOG2E;
  float p[8];
#pragma unroll
  for (int e = 0; e < 4; ++e) {
    p[e] = __builtin_amdgcn_exp2f(fmaf(s0[e], SCALE_LOG2E, -mt));
    p[4 + e] = __builtin_amdgcn_exp2f(fmaf(s1[e], SCALE_LOG2E, -mt));
  }
  const u32x4 pp = {pk2(p[0], p[1]), pk2(p[2], p[3]), pk2(p[4], p[5]), pk2(p[6], p[7])};
  f32x4 o = {0.f, 0.f, 0.f, 0.f};
#pragma unroll
  for (int m = 0; m < 8; ++m) {
    const bool on = jm == m;
    const u32x4 b = {on ? pp.x : 0u, on ? pp.y : 0u, on ? pp.z : 0u, on ? pp.w : 0u};
    o = mfma32(cat8(t.v[2 * m], t.v[2 * m + 1]), __builtin_bit_cast(bf16x8, b), o);
  }
  const float mn = max2(st.m, mt);
  const float fa = __builtin_amdgcn_exp2f(st.m - mn), fb = __builtin_amdgcn_exp2f(mt - mn);
  st.l = fmaf(st.l, fa, (((p[0] + p[1]) + (p[2] + p[3])) + ((p[4] + p[5]) + (p[6] + p[7]))) * fb);
  st.m = mn;
#pragma unroll
  for (int e = 0; e < 4; ++e) st.acc[e] = fmaf(st.acc[e], fa, o[e] * fb);
}

#define STAMP(i) do { } while (0)
#define CSTAMP(i) do { } while (0)
#define STAMP_DUMP() do { } while (0)
#define STAMP_INIT() do { } while (0)

DEV int lane_id() { int l; asm volatile("v_mbcnt_lo_u32_b32 %0, -1, 0\n\tv_mbcnt_hi_u32_b32 %0, -1, %0" : "=v"(l)); __builtin_assume(l >= 0 && l < 64); return l; }
typedef const __attribute__((address_space(4))) Params* kargs_t;
DEV kargs_t kargs() { kargs_t q = (kargs_t)__builtin_amdgcn_kernarg_segment_ptr(); asm volatile("" : "+s"(q)); return q; }
#define PHASE() \
  asm volatile("" : "+s"(wave), "+s"(b), "+s"(layer), "+s"(ws)); \
  const int lane = lane_id(), g = lane >> 4, j = lane & 15; \
  const int hk = b >> 4, sp = b & 15; \
  const bool both = sp == 7 || sp == 8; \
  const int gu = both ? 1000 : hk * 14 + (sp < 7 ? sp : sp - 2); \
  const rsrc_t rA = mk_rsrc(ws + WS_ARENA, ARENA_BYTES); \
  (void)g; (void)j; (void)hk; (void)sp; (void)both; (void)gu; (void)rA

template <int NPIECE> DEV void load_slice(const u16* w, u32 wbytes, u32 rowbytes, u32 off0, u32 lds, int wave, int lane) {
  const rsrc_t r = mk_rsrc(w, wbytes);
  const u32 vo = (lane >> 3) * rowbytes + (((lane & 7) ^ (((lane >> 4) & 3) << 1)) << 4);
#pragma unroll
  for (int i = 0; i < (NPIECE + 3) / 4; ++i)
    if (4 * i + 3 < NPIECE || 4 * i + wave < NPIECE) dma1k(r, vo, off0 + (4 * i + wave) * 8 * rowbytes, lds + (4 * i + wave) * 1024);
}
DEV void load_wq(const u16* wq, int b, bool both, int wave, int lane) {
  load_slice<18>(wq, D * HQ * DH * 2, HQ * DH * 2, (b & 3) * 144 * (HQ * DH * 2) + (b >> 2) * 128, both ? L_R2 : L_R1, wave, lane);
}
DEV void load_wo(const u16* wo, int b, int wave, int lane) {
  load_slice<32>(wo, HQ * DH * D * 2, D * 2, (b >> 4) * 256 * (D * 2) + (b & 15) * 128, L_R2, wave, lane);
}
DEV void load_gu(const u16* w, int gu, u32 lds, int wave, int lane) {
  load_slice<24>(w, D * FF * 2, FF * 2, (gu % 3) * 192 * (FF * 2) + (gu / 3) * 128, lds, wave, lane);
}
DEV void load_alias(int wave, int b) {
  asm volatile("" : "+s"(wave), "+s"(b));
  const int lane = lane_id(), hk = b >> 4, sp = b & 15;
  const int gu = hk * 14 + (sp < 7 ? sp : sp - 2);
  if (sp == 7 || sp == 8) load_wo(kargs()->wo, b, wave, lane);
  else if (gu < 216) load_gu(kargs()->wg, gu, L_R1, wave, lane);
}
DEV u32 slice_addr(u32 base, u32 row, int j) { return base + row * 128 + (((row >> 1) & 3) << 5) + (j & 3) * 8; }
DEV void ln_partial(float s, float q, int wave, int lane) {
  s += dpp<0x128>(s); q += dpp<0x128>(q); s += dpp<0x124>(s); q += dpp<0x124>(q);
  s += dpp<0x122>(s); q += dpp<0x122>(q); s += dpp<0x121>(s); q += dpp<0x121>(q);
  auto x = __builtin_amdgcn_permlane16_swap(fbits(s), fbits(q), false, false);
  const float h = bitsf(x[0]) + bitsf(x[1]);
  auto y = __builtin_amdgcn_permlane32_swap(fbits(h), fbits(h), false, false);
  const float r = bitsf(y[0]) + bitsf(y[1]);
  if ((lane & 47) == 0) sts32(L_ST + wave * 8 + (lane >> 2), fbits(r));
}
DEV void ln_stats(float& mb, float& inv) {
  const u32x4 a = lds128(L_ST), b = lds128(L_ST + 16);
  const float c = ldsf(L_ST + 32);
  const float ms = ((bitsf(a.x) + bitsf(a.z)) + (bitsf(b.x) + bitsf(b.z))) * (1.0f / D);
  const float mq = ((bitsf(a.y) + bitsf(a.w)) + (bitsf(b.y) + bitsf(b.w))) * (1.0f / D);
  const float var = fmaxf(mq - ms * ms, 0.f);
  mb = rb(c + ms);
  inv = __builtin_amdgcn_rcpf(rb(__builtin_amdgcn_sqrtf(rb(rb(var) + 1e-5f))));
}
DEV void publish_cols(rsrc_t rA, u32 rec, const f32x4 (&ac)[4], int wave, int g, int j) {
  f32x4 a;
#pragma unroll
  for (int e = 0; e < 4; ++e) {
    const float lo = (j & 1) ? ac[1][e] : ac[0][e], hi = (j & 1) ? ac[3][e] : ac[2][e];
    a[e] = (j & 2) ? hi : lo;
  }
  if (wave != 0 && j < 4) sts128(L_PP + (wave - 1) * 256 + j * 64 + g * 16, __builtin_bit_cast(u32x4, a));
  bar();
  if (wave == 0 && j < 4) {
    const u32 o = L_PP + j * 64 + g * 16;
    const f32x4 p0 = __builtin_bit_cast(f32x4, lds128(o)), p1 = __builtin_bit_cast(f32x4, lds128(o + 256)), p2 = __builtin_bit_cast(f32x4, lds128(o + 512));
    st_sc1(rA, rec + j * 64 + g * 16, canon4((a + p0) + (p1 + p2)));
  }
}
DEV bool is_nan(float x) { return x != x; }
DEV f32x4 asf(u32x4 v) { return __builtin_bit_cast(f32x4, v); }
template <int SLP> DEV void gather_cols(rsrc_t rA, int use, u32 first_poll, int wave, int lane) {
  const int g = lane >> 4, j = lane & 15;
  const u32 base = A_P2 + use * (144 * 256);
  const u32 o0 = base + g * 2304 + wave * 512 + j * 16;
  const u32 o2 = base + (4 * g + (j >> 2)) * 2304 + 2048 + wave * 64 + (j & 3) * 16;
  const u32 xa = L_XV + (g < 2 ? 32 * wave + 16 * g + j : 128 + 4 * wave + (j & 3)) * 8;
  const bool mine = g < 2 || (g == 2 && j < 4);
  const u32x2 xr = lds64(xa);
  f32x4 xo = {bflo(xr.x), bfhi(xr.x), bflo(xr.y), bfhi(xr.y)};
  float c = ldsf(L_ST + 32);
  asm volatile("" : "+v"(xo), "+v"(c));
  u32x4 t[9];
  u64 m[9];
  float s[4];
#pragma unroll
  for (int i = 0; i < 9; ++i) { t[i] = SENT4; m[i] = ~0ull; }
  wait_until(first_poll);
  _Pragma("nounroll") for (u32 spin = 0; spin < SPIN_CAP; ++spin) {
    asm volatile(PL("buffer_load_dwordx4", "%8", "%[c]", "0", "0", "%[m8]")
                 PL("buffer_load_dwordx4", "%0", "%[a]", "0", "0", "%[m0]") PL("buffer_load_dwordx4", "%1", "%[a]", "%[s1]", "0", "%[m1]")
                 PL("buffer_load_dwordx4", "%2", "%[a]", "%[s2]", "0", "%[m2]") PL("buffer_load_dwordx4", "%3", "%[a]", "%[s3]", "0", "%[m3]")
                 PL("buffer_load_dwordx4", "%4", "%[a]", "0", "256", "%[m4]") PL("buffer_load_dwordx4", "%5", "%[a]", "%[s1]", "256", "%[m5]")
                 PL("buffer_load_dwordx4", "%6", "%[a]", "%[s2]", "256", "%[m6]") PL("buffer_load_dwordx4", "%7", "%[a]", "%[s3]", "256", "%[m7]")
                 PL_END
                 : "+v"(t[0]), "+v"(t[1]), "+v"(t[2]), "+v"(t[3]), "+v"(t[4]), "+v"(t[5]), "+v"(t[6]), "+v"(t[7]), "+v"(t[8])
                 : [a] "v"(o0), [c] "v"(o2), [r] "s"(rA), [s1] "s"(9216), [s2] "s"(18432), [s3] "s"(27648),
                   [m0] "s"(m[0]), [m1] "s"(m[1]), [m2] "s"(m[2]), [m3] "s"(m[3]), [m4] "s"(m[4]), [m5] "s"(m[5]), [m6] "s"(m[6]),
                   [m7] "s"(m[7]), [m8] "s"(m[8])
                 : "memory");
    const f32x4 a0 = (asf(t[0]) + asf(t[1])) + (asf(t[2]) + asf(t[3]));
    const f32x4 a1 = (asf(t[4]) + asf(t[5])) + (asf(t[6]) + asf(t[7]));
#pragma unroll
    for (int e = 0; e < 4; ++e) {
      float a2 = bitsf(t[8][e]);
      a2 += dpp<0x124>(a2); a2 += dpp<0x128>(a2);
      s[e] = xrow_sum4(a0[e], a1[e], a2, a2);
    }
    if (!any_lane(is_nan((s[0] + s[1]) + (s[2] + s[3])))) break;
    u64 any = 0;
#pragma unroll
    for (int i = 0; i < 9; ++i) { m[i] = pend4(t[i]); any |= m[i]; }
    if (any == 0) break;
    asm volatile("s_sleep %0" ::"i"(SLP) : "memory");
  }
  const u32 r0 = pk2(s[0], s[1]), r1 = pk2(s[2], s[3]);
  const u32x2 xn = {pk2(xo[0] + bflo(r0), xo[1] + bfhi(r0)), pk2(xo[2] + bflo(r1), xo[3] + bfhi(r1))};
  if (mine) sts64(xa, xn);
  const float d0 = bflo(xn.x) - c, d1 = bfhi(xn.x) - c, d2 = bflo(xn.y) - c, d3 = bfhi(xn.y) - c;
  ln_partial(mine ? (d0 + d1) + (d2 + d3) : 0.f, mine ? fmaf(d0, d0, d1 * d1) + fmaf(d2, d2, d3 * d3) : 0.f, wave, lane);
}
DEV void final_cols(rsrc_t rA, int cg, u32 first_poll, int lane) {
  const int g = lane >> 4, j = lane & 15;
  const u32 o0 = A_P2 + 3 * (144 * 256) + g * 2304 + cg * 256 + j * 16;
  const float xo = bflo(*(const LAS u16*)(size_t)(L_XV + cg * 128 + j * 8 + g * 2));
  GAS u16* op = (GAS u16*)kargs()->out + cg * 64 + j * 4 + g;
  u32x4 t[4] = {SENT4, SENT4, SENT4, SENT4};
  u64 m0 = ~0ull;
  float s;
  wait_until(first_poll);
  _Pragma("nounroll") for (u32 spin = 0; spin < SPIN_CAP; ++spin) {
    asm volatile("s_mov_b64 exec, %[m0]\n\t"
                 "buffer_load_dwordx4 %0, %[a], %[r], 0 offen sc1\n\t"
                 "buffer_load_dwordx4 %1, %[a], %[r], %[s1] offen sc1\n\t"
                 "buffer_load_dwordx4 %2, %[a], %[r], %[s2] offen sc1\n\t"
                 "buffer_load_dwordx4 %3, %[a], %[r], %[s3] offen sc1\n\t" PL_END
                 : "+v"(t[0]), "+v"(t[1]), "+v"(t[2]), "+v"(t[3])
                 : [a] "v"(o0), [r] "s"(rA), [m0] "s"(m0), [s1] "s"(9216), [s2] "s"(18432), [s3] "s"(27648) : "memory");
    const f32x4 a0 = (asf(t[0]) + asf(t[1])) + (asf(t[2]) + asf(t[3]));
    s = xrow_sum4(a0[0], a0[1], a0[2], a0[3]);
    if (!any_lane(is_nan(s))) break;
    m0 = __builtin_amdgcn_ballot_w64(umax(umax5(umax5(0, t[0]), t[1]), umax5(umax5(0, t[2]), t[3])) == SENT);
    if (m0 == 0) break;
    poll_pause();
  }
  *op = (u16)pk2(xo + rb(s), 0.f);
}
DEV u32 claim_issue(int n, int lim, rsrc_t rA, u32 off) {
  u32 ret;
  asm volatile("v_mov_b32 %0, 1\n\t"
               "s_cmp_lt_i32 %[n], %[lim]\n\ts_cbranch_scc1 .Lclaim_skip%=\n\t"
               "s_mov_b64 exec, 1\n\t"
               "buffer_atomic_add %0, off, %[r], %[o] sc0\n\t"
               "s_mov_b64 exec, -1\n"
               ".Lclaim_skip%=:"
               : "=&v"(ret) : [n] "s"(n), [lim] "s"(lim), [r] "s"(rA), [o] "s"(off) : "memory", "scc");
  return ret;
}

#define STREAM(PK, PV) \
  Stream sr; \
  sr.rK = mk_rsrc((PK) + (size_t)hk * SK * DH, SK * DH * 2); \
  sr.rV = mk_rsrc((PV) + (size_t)hk * SK * DH, SK * DH * 2); \
  sr.dv0 = g * 256 + ((j ^ (g << 1)) << 4); sr.dv1 = sr.dv0 ^ 128;                \
  sr.slot = L_RING + wave * 16384; \
  sr.ka = sr.slot + j * 256 + ((g ^ ((j & 7) << 1)) << 4);                        \
  sr.va = sr.slot + 8192 + (4 * g + (j >> 2)) * 256 + ((((4 * g + (j >> 2)) & 7)) << 5) + (j & 3) * 8; \
  const u32 tile0 = sp * (1u << 20) + wave * 8192
#define MOVE_TILES(T0, T1) \
  if (layer == 0) { \
    PHASE(); \
    STREAM(kargs()->k, kargs()->v); \
    _Pragma("unroll") for (int t = (T0); t < (T1); ++t) move_tile<12, true>(sr, st[t], tile0 + (t + 1) * TSTEP, true, 0); \
  }

__global__ void __launch_bounds__(256, 1) fused_decode(Params p) {
  __shared__ __attribute__((aligned(256))) unsigned char smem[163840];
  asm volatile("" ::"v"((u32)(size_t)(LAS unsigned char*)smem));
  int wave = rfl(threadIdx.x >> 6);
  int b = blockIdx.x;
  int layer = 0;
  unsigned char* ws = p.ws;
  STAMP_INIT();
  STAMP(0);

  u32 ep_lo, ep_hi;
  Tile st[NR];
  {
    PHASE();
    STREAM(p.k, p.v);
    issue_tile(sr, tile0, 0);
    STAMP(25);
    u32x2 ep; u32x4 xa; u32 x0;
    {
      const rsrc_t rX = mk_rsrc(p.x, D * 2);
      const rsrc_t rE = mk_rsrc(ws + WS_EPOCH, 8);
      const u32 o1 = (wave * 64 + lane) * 16;
      asm volatile("buffer_load_dwordx2 %0, off, %3, 0 sc1\n\t"
                   "buffer_load_dwordx4 %1, %4, %5, 0 offen nt\n\t"
                   "buffer_load_dword %2, off, %5, 0 nt"
                   : "=&v"(ep), "=&v"(xa), "=&v"(x0) : "s"(rE), "v"(o1), "s"(rX) : "memory");
    }
    load_wq(p.wq, b, both, wave, lane);
    st_sc1(rA, b * 5120 + wave * 1024 + lane * 16, SENT4);
    if (wave == 0) st_sc1(rA, b * 5120 + 4096 + lane * 16, SENT4);
    if (sp < 7) load_wo(p.wo, b, wave, lane);
    else load_slice<36>(p.wd, FF * D * 2, D * 2, hk * 288 * (D * 2) + (sp - 7) * 128, both ? L_R1 : L_R2, wave, lane);
    if (gu < 216) load_gu(p.wu, gu, L_R1 + 24576, wave, lane);
    if (wave == 3 && lane < 24) sts128(L_ONES + lane * 16, u32x4{0x3F803F80u, 0x3F803F80u, 0x3F803F80u, 0x3F803F80u});
    asm volatile("s_waitcnt vmcnt(8)" : "+v"(ep), "+v"(xa), "+v"(x0) :: "memory");
    move_tile<63, true>(sr, st[0], tile0 + TSTEP, true, 0);
    {
      const bool mine = wave * 64 + lane < 72;
      const float c = bflo(x0);
      float sx = 0.f, sq = 0.f;
#pragma unroll
      for (int e = 0; e < 4; ++e) { const float d0 = bflo(xa[e]) - c, d1 = bfhi(xa[e]) - c; sx += d0 + d1; sq = fmaf(d0, d0, fmaf(d1, d1, sq)); }
      if (mine) sts128(L_XV + (wave * 64 + lane) * 16, xa);
      if (wave == 0 && lane == 0) sts32(L_ST + 32, fbits(c));
      ln_partial(mine ? sx : 0.f, mine ? sq : 0.f, wave, lane);
    }
    const u64 e1 = (((u64)rfl(ep.y) << 32) | rfl(ep.x)) + 1;
    ep_lo = (u32)e1; ep_hi = (u32)(e1 >> 32);
  }

  bf16x8 wf[5];
  auto load_wq_frags = [&]() {
    PHASE();
    const u32 wb = both ? L_R2 : L_R1;
    const u32 ta = slice_addr(wb, 8 * g + (j >> 2), j) ^ (wave << 5), tb = slice_addr(wb, 8 * g + 4 + (j >> 2), j) ^ (wave << 5);
#pragma unroll
    for (int i = 0; i < 4; ++i) wf[i] = cat8(ldstr(ta + i * 4096), ldstr(tb + i * 4096));
    const s16x4 w4 = ldstr(slice_addr(wb, 128 + 4 * g + (j >> 2), j) ^ (wave << 5));
    wf[4] = cat8(w4, w4);
  };
  bar();
  load_wq_frags();
  u32 tref = now() - 1000;

  for (;;) {
    f32x4 qp;
    {
      PHASE();
      STAMP(layer == 0 ? 1 : 13);
      bar();
      GAS u64* fl_ptr = (GAS u64*)(ws + WS_FLAGS) + b;
      asm volatile("" : "+s"(ep_lo), "+s"(ep_hi));
      if (layer == 0 && wave == 0 && lane == 0)
        __hip_atomic_store(fl_ptr, ((u64)ep_hi << 32) | ep_lo, __ATOMIC_RELAXED, __HIP_MEMORY_SCOPE_AGENT);
      const u32 xn = j == 1 ? L_ONES : L_XV + (b & 3) * 288;
      bf16x8 xf[4];
#pragma unroll
      for (int i = 0; i < 4; ++i) xf[i] = __builtin_bit_cast(bf16x8, lds128(xn + i * 64 + g * 16));
      const u32x2 x4 = lds64(xn + 256 + g * 8);
      float mb, inv;
      ln_stats(mb, inv);
      f32x4 aq = mfma16(__builtin_bit_cast(s16x4, __builtin_bit_cast(u32x4, wf[4]).xy), __builtin_bit_cast(s16x4, x4), f32x4{0.f, 0.f, 0.f, 0.f});
#pragma unroll
      for (int i = 0; i < 4; ++i) aq = mfma32(wf[i], xf[i], aq);
#pragma unroll
      for (int e = 0; e < 4; ++e) qp[e] = (aq[e] - mb * dpp<0xB1>(aq[e])) * inv;
    }
    MOVE_TILES(1, (NR + 1) / 2);
    {
      PHASE();
      if (layer == 0) {
        const rsrc_t rF = mk_rsrc(ws + WS_FLAGS, NWG * 8);
        _Pragma("nounroll") for (u32 spin = 0; spin < SPIN_CAP; ++spin) {
          const u32x4 f0 = ld_sc1(rF, lane * 32), f1 = ld_sc1(rF, lane * 32 + 16);
          const u32 bad = ((f0.x ^ ep_lo) | (f0.y ^ ep_hi)) | ((f0.z ^ ep_lo) | (f0.w ^ ep_hi)) | ((f1.x ^ ep_lo) | (f1.y ^ ep_hi)) | ((f1.z ^ ep_lo) | (f1.w ^ ep_hi));
          if (!any_lane(bad != 0)) break;
          poll_pause();
        }
      }
      if (j == 0) st_sc1(rA, A_Q + (layer * 256 + b) * 256 + wave * 64 + g * 16, canon4(qp));
      STAMP(layer == 0 ? 2 : 14);
    }
    MOVE_TILES((NR + 1) / 2, NR);

    Soft sm;
    {
      PHASE();
      STREAM(kargs()->k, kargs()->v);
      const int jm = j >> 1;
      const int ins = layer == 0 ? 1000 : L2POS;
      auto own = [&](int r) { return sp * 32 + (layer == 0 ? NR + 1 + r : r < ins ? NR + r : r < ins + NL2 ? NSTEP - 1 - NL2 + r - ins : NR + r - NL2); };
      constexpr u32 L2BITS = (((1u << NL2) - 1u) << (NSTEP - 1 - NL2)) | KEEPALL;
      const u32 kgate = layer == 0 ? 0x7FFFFFFFu : 0u;
      auto keep_of = [&](u32 blk) { return (((KEEPMASK >> ((blk + (blk >> 5) * KEEPMUL + hk) & 31)) & (KEEPLIM >> (blk & 31))) | (L2BITS >> (blk & 31))) & (kgate >> (blk & 31)) & 1; };
      Tile tp;
      move_tile<12, false>(sr, tp, own(0) * TSTEP + wave * 8192, layer == 0, keep_of(own(0)));
      if (wave == 0) {
        const u32 off = A_Q + (layer * 256 + 16 * hk + g) * 256 + j * 16;
        u32x4 t[4] = {SENT4, SENT4, SENT4, SENT4};
        u64 m0 = ~0ull;
        float qv[4];
        wait_until(tref + DLY_Q);
        _Pragma("nounroll") for (u32 spin = 0; spin < SPIN_CAP; ++spin) {
          asm volatile("s_mov_b64 exec, %[m0]\n\t"
                       "buffer_load_dwordx4 %0, %[a], %[r], 0 offen sc1\n\t"
                       "buffer_load_dwordx4 %1, %[a], %[r], 0 offen offset:1024 sc1\n\t"
                       "buffer_load_dwordx4 %2, %[a], %[r], 0 offen offset:2048 sc1\n\t"
                       "buffer_load_dwordx4 %3, %[a], %[r], 0 offen offset:3072 sc1\n\t" PL_END
                       : "+v"(t[0]), "+v"(t[1]), "+v"(t[2]), "+v"(t[3]) : [a] "v"(off), [r] "s"(rA), [m0] "s"(m0) : "memory");
#pragma unroll
          for (int i = 0; i < 4; ++i) qv[i] = xrow_sum4(bitsf(t[i].x), bitsf(t[i].y), bitsf(t[i].z), bitsf(t[i].w));
          if (!any_lane(is_nan((qv[0] + qv[1]) + (qv[2] + qv[3])))) break;
          m0 = __builtin_amdgcn_ballot_w64(umax(umax5(umax5(0, t[0]), t[1]), umax5(umax5(0, t[2]), t[3])) == SENT);
          if (m0 == 0) break;
          poll_pause();
        }
        if (layer == 1) issue_tile(sr, own(0) * TSTEP, 0);
        const u32 q01 = pk2(qv[0], qv[1]), q23 = pk2(qv[2], qv[3]);
        const u32 qa = L_QB + j * 8 + g * 2;
        *(LAS u16*)(size_t)qa = (u16)q01; *(LAS u16*)(size_t)(qa + 128) = (u16)(q01 >> 16);
        *(LAS u16*)(size_t)(qa + 256) = (u16)q23; *(LAS u16*)(size_t)(qa + 384) = (u16)(q23 >> 16);
      }
      STAMP(layer == 0 ? 3 : 15);
      bf16x8 q[4];
      {
        bar();
        if (wave != 0 && layer == 1) issue_tile(sr, own(0) * TSTEP + wave * 8192, 0);
#pragma unroll
        for (int m = 0; m < 4; ++m) q[m] = __builtin_bit_cast(bf16x8, lds128(L_QB + (j & 1) * 256 + m * 64 + g * 16));
      }
      STAMP(layer == 0 ? 4 : 16);
      if (layer == 1) CSTAMP(28);

      sm.acc = f32x4{0.f, 0.f, 0.f, 0.f}; sm.m = -INFINITY; sm.l = 0.f;
      load_alias(wave, b);
      attend(tp, q, jm, sm);
      const int nst = layer == 0 ? NS0 - NR - 1 : NS1 - NR + NL2;
      const u32 npool = layer == 0 ? 2 * (NSTEP - 1 - NL2 - NS0) : 16 * (NSTEP - 1 - NL2 - NS1);
      int pos = 1;
      u32 nx = own(1);
      int tail = layer == 0 ? NSTEP - 1 - NL2 : NSTEP;
      int mine = 0;
      int lim = wave == 0 && npool != 0 ? nst - 1 : 1000;
      const u32 cl_off = A_CLAIM + (layer == 0 ? (b & ~3) + ((b ^ (b >> 1)) & 1) : 256 + hk) * 128;
      const int sphase = b & 1;
      _Pragma("nounroll") for (int it = 0;; ++it) {
        const bool more = nx != NONE;
        u32 claimv = claim_issue(pos, lim, rA, cl_off);
        Tile tl;
        move_tile<12, false>(sr, tl, nx * TSTEP + wave * 8192, more, keep_of(nx));
        if (wave == 0) {
          u32 ny;
          if (pos + 1 < nst) { ++pos; ny = own(pos); }
          else {
            pos = nst;
            asm volatile("" : "+v"(claimv) :: "memory");
            const u32 c = rfl(claimv) + 1;
            if (lim != 1000 && c < npool) {
              constexpr int P0 = NSTEP - 1 - NL2 - NS0;
              ny = layer == 1 ? (c & 15) * 32 + NS1 + (c >> 4) : mine < P0 ? sp * 32 + NS0 + mine : (sp ^ 3) * 32 + NS0 + 2 * P0 - 1 - mine;
              ++mine;
            } else {
              lim = 1000;
              ny = tail < NSTEP ? sp * 32 + tail : NONE;
              ++tail;
            }
          }
          sts32(L_NX + (it & 1) * 4, ny);
        }
        attend(tl, q, jm, sm);
#pragma unroll
        for (int t = 0; t < NR; ++t)
          if (it == 2 * t + sphase) attend(st[t], q, jm, sm);
        if (!more) break;
        bar();
        nx = rfl(lds32(L_NX + (it & 1) * 4));
      }
      STAMP(layer == 0 ? 5 : 17);
      if (layer == 0) CSTAMP(27); else CSTAMP(29);
    }
    bf16x8 gf[12], af[8];
    {
      PHASE();
      const float lt = xrow_sum(sm.l);
      if (wave != 0) sts128(L_CMB + (wave - 1) * 1024 + lane * 16, __builtin_bit_cast(u32x4, sm.acc));
      if (lane < 2) sts64(L_CML + wave * 16 + lane * 8, u32x2{fbits(sm.m), fbits(lt)});
      bar();
      if (wave == 0) {
        float mw[4], lw[4];
#pragma unroll
        for (int w = 0; w < 4; ++w) { const u32x2 t = lds64(L_CML + w * 16 + (j & 1) * 8); mw[w] = bitsf(t.x); lw[w] = bitsf(t.y); }
        const float M = max2(max2(mw[0], mw[1]), max2(mw[2], mw[3]));
        float sc[4], L = 0.f;
#pragma unroll
        for (int w = 0; w < 4; ++w) { sc[w] = __builtin_amdgcn_exp2f(mw[w] - M); L = fmaf(sc[w], lw[w], L); }
        f32x4 o = sm.acc * sc[0];
#pragma unroll
        for (int w = 1; w < 4; ++w) o += __builtin_bit_cast(f32x4, lds128(L_CMB + (w - 1) * 1024 + lane * 16)) * sc[w];
        const u32 rec = A_P1 + (layer * 256 + b) * 1152;
        st_sc1(rA, rec + (j & 1) * 512 + (j >> 1) * 64 + g * 16, canon4(o));
        if (lane < 2) st_sc1_64(rA, rec + 1024 + lane * 8, u32x2{canon(fbits(M)), canon(fbits(L))});
      }
      STAMP(layer == 0 ? 6 : 18);
      {
        const u32 ta = slice_addr(L_R1, 8 * g + (j >> 2), j) ^ (wave << 5), tb = slice_addr(L_R1, 8 * g + 4 + (j >> 2), j) ^ (wave << 5);
#pragma unroll
        for (int i = 0; i < 12; ++i) gf[i] = cat8(ldstr(ta + i * 4096), ldstr(tb + i * 4096));
        const u32 tc = slice_addr(L_R2, 64 * wave + 4 * g + (j >> 2), j);
#pragma unroll
        for (int i = 0; i < 8; ++i) af[i] = cat8(ldstr((tc ^ ((i & 3) << 5)) + (i >> 2) * 4096), ldstr((tc ^ ((i & 3) << 5)) + (i >> 2) * 4096 + 2048));
      }
      bar();
      if (layer == 0) load_wq(kargs()->wq, b, both, wave, lane);
    }
    {
      PHASE();
      if (sp < 9) {
        u32x4 t[4] = {SENT4, SENT4, SENT4, SENT4}; u32x2 ml = {SENT, SENT};
        const u32 rec = A_P1 + (layer * 256 + 16 * hk + j) * 1152;
        const u32 o1 = rec + wave * 256 + g * 16, o2 = rec + 1024 + (wave >> 1) * 8;
        u64 m0 = H1LIGHT ? 0ull : ~0ull, m1 = ~0ull;
        _Pragma("nounroll") for (u32 spin = 0; spin < SPIN_CAP; ++spin) {
          asm volatile("s_mov_b64 exec, %[m1]\n\t"
                       "buffer_load_dwordx2 %4, %[b], %[r], 0 offen sc1\n\t"
                       "s_mov_b64 exec, %[m0]\n\t"
                       "buffer_load_dwordx4 %0, %[a], %[r], 0 offen sc1\n\t"
                       "buffer_load_dwordx4 %1, %[a], %[r], 0 offen offset:64 sc1\n\t"
                       "buffer_load_dwordx4 %2, %[a], %[r], 0 offen offset:128 sc1\n\t"
                       "buffer_load_dwordx4 %3, %[a], %[r], 0 offen offset:192 sc1\n\t" PL_END
                       : "+v"(t[0]), "+v"(t[1]), "+v"(t[2]), "+v"(t[3]), "+v"(ml)
                       : [a] "v"(o1), [b] "v"(o2), [r] "s"(rA), [m0] "s"(m0), [m1] "s"(m1) : "memory");
          m1 = __builtin_amdgcn_ballot_w64(umax(ml.x, ml.y) == SENT);
          m0 = __builtin_amdgcn_ballot_w64(umax(umax5(umax5(0, t[0]), t[1]), umax5(umax5(0, t[2]), t[3])) == SENT);
          if ((m0 | m1) == 0) break;
          if (H1LIGHT) m0 &= ~m1;
          if (m0 == 0 || !H1LIGHT) asm volatile("s_sleep %0" ::"i"(SLP_L) : "memory");
        }
        STAMP(layer == 0 ? 7 : 19);
        float M = bitsf(ml.x);
        M = max2(M, dpp<0x128>(M)); M = max2(M, dpp<0x124>(M)); M = max2(M, dpp<0x122>(M)); M = max2(M, dpp<0x121>(M));
        const float w0 = __builtin_amdgcn_exp2f(bitsf(ml.x) - M);
        float ov[16];
#pragma unroll
        for (int i = 0; i < 16; ++i) ov[i] = w0 * bitsf(t[i >> 2][i & 3]);
        const float inv = __builtin_amdgcn_rcpf(row_sum(w0 * bitsf(ml.y)));
        row_sum16(ov);
        f32x4 ac[4];
#pragma unroll
        for (int kk = 0; kk < 2; ++kk) {
          const u32x4 ob = {pk2(ov[8 * kk] * inv, ov[8 * kk + 1] * inv), pk2(ov[8 * kk + 2] * inv, ov[8 * kk + 3] * inv),
                            pk2(ov[8 * kk + 4] * inv, ov[8 * kk + 5] * inv), pk2(ov[8 * kk + 6] * inv, ov[8 * kk + 7] * inv)};
#pragma unroll
          for (int c = 0; c < 4; ++c) ac[c] = mfma32(af[4 * kk + c], __builtin_bit_cast(bf16x8, ob), kk ? ac[c] : f32x4{0.f, 0.f, 0.f, 0.f});
        }
        publish_cols(rA, A_P2 + ((2 * layer) * 144 + hk * 9 + sp) * 256, ac, wave, g, j);
        STAMP(layer == 0 ? 8 : 20);
      }
    }
    {
      PHASE();
      gather_cols<SLP_X1>(rA, 2 * layer, now() + (sp < 9 ? DLY_X1 : DLY_X1N), wave, lane);
      STAMP(layer == 0 ? 9 : 21);
      tref = now();
      bar();
      if (gu < 216) {
        const u32 xn = (j == 1 ? L_ONES : L_XV + (gu % 3) * 384) + g * 16;
        bf16x8 xf[6];
#pragma unroll
        for (int i = 0; i < 6; ++i) xf[i] = __builtin_bit_cast(bf16x8, lds128(xn + i * 64));
        float mb, inv;
        ln_stats(mb, inv);
        f32x4 ag = {0.f, 0.f, 0.f, 0.f}, au = ag;
#pragma unroll
        for (int i = 0; i < 6; ++i) { ag = mfma32(gf[i], xf[i], ag); au = mfma32(gf[6 + i], xf[i], au); }
        f32x4 r;
#pragma unroll
        for (int e = 0; e < 4; ++e) {
          const float sg = dpp<0xB1>(ag[e]), su = dpp<0xB1>(au[e]);
          r[e] = ((j == 0 ? ag[e] : su) - mb * (j == 0 ? sg : au[e])) * inv;
        }
        if (j < 2) st_sc1(rA, A_G + (layer * 216 + gu) * 512 + wave * 128 + j * 64 + g * 16, canon4(r));
      }
      STAMP(layer == 0 ? 10 : 22);
    }
    {
      PHASE();
      if (sp >= 7) {
        bf16x8 df[12];
        const u32 wb = both ? L_R1 : L_R2;
        const u32 ta = slice_addr(wb, 64 * wave + 8 * g + (j >> 2), j), tb = slice_addr(wb, 64 * wave + 8 * g + 4 + (j >> 2), j);
#pragma unroll
        for (int i = 0; i < 12; ++i) {
          const u32 ko = i < 8 || wave == 3 ? (i >> 2) * 4096 : 0;
          df[i] = cat8(ldstr((ta ^ ((i & 3) << 5)) + ko), ldstr((tb ^ ((i & 3) << 5)) + ko));
        }
        u32x4 t[4] = {SENT4, SENT4, SENT4, SENT4};
        const u32 u1 = 288 * hk + 64 * wave + 4 * j, u2 = 288 * hk + 256 + 4 * (j & 7);
        const u32 gb = A_G + (layer * 216 + g) * 512;
        const u32 o1 = g < 3 ? gb + (u1 >> 6) * 1536 + ((u1 >> 4) & 3) * 128 + (u1 & 15) * 4 : OOB;
        const u32 o2 = g < 3 && wave == 3 ? gb + (u2 >> 6) * 1536 + ((u2 >> 4) & 3) * 128 + (u2 & 15) * 4 : OOB;
        u64 m0 = ~0ull;
        float hs[4];
        wait_until(tref + DLY_H);
        _Pragma("nounroll") for (u32 spin = 0; spin < SPIN_CAP; ++spin) {
          asm volatile("s_mov_b64 exec, %[m0]\n\t"
                       "buffer_load_dwordx4 %0, %[a], %[r], 0 offen sc1\n\t"
                       "buffer_load_dwordx4 %1, %[a], %[r], 0 offen offset:64 sc1\n\t"
                       "buffer_load_dwordx4 %2, %[b], %[r], 0 offen sc1\n\t"
                       "buffer_load_dwordx4 %3, %[b], %[r], 0 offen offset:64 sc1\n\t" PL_END
                       : "+v"(t[0]), "+v"(t[1]), "+v"(t[2]), "+v"(t[3])
                       : [a] "v"(o1), [b] "v"(o2), [r] "s"(rA), [m0] "s"(m0) : "memory");
#pragma unroll
          for (int i = 0; i < 4; ++i) hs[i] = xrow_sum4(bitsf(t[i].x), bitsf(t[i].y), bitsf(t[i].z), bitsf(t[i].w));
          if (!any_lane(is_nan((hs[0] + hs[1]) + (hs[2] + hs[3])))) break;
          m0 = __builtin_amdgcn_ballot_w64(umax(umax5(umax5(0, t[0]), t[1]), umax5(umax5(0, t[2]), t[3])) == SENT);
          if (m0 == 0) break;
          poll_pause();
        }
        STAMP(layer == 0 ? 11 : 23);
        const u32 gp = pk2(hs[0], hs[2]), up = pk2(hs[1], hs[3]);
        const float g1 = bflo(gp), g2 = bfhi(gp);
        const u32 hp = pk2((g1 * (g1 > 0.f ? 1.f : 0.f)) * bflo(up), (g2 * (g2 > 0.f ? 1.f : 0.f)) * bfhi(up));
        const u16 h1 = (u16)hp, h2 = (u16)(hp >> 16);
        *(LAS u16*)(size_t)(L_HB + wave * 192 + j * 8 + g * 2) = h1;
        if (j < 8) *(LAS u16*)(size_t)(L_HB + wave * 192 + 128 + j * 8 + g * 2) = h2;
        f32x4 ac[4];
#pragma unroll
        for (int kk = 0; kk < 3; ++kk) {
          const bf16x8 hb = __builtin_bit_cast(bf16x8, lds128(L_HB + wave * 192 + kk * 64 + g * 16));
#pragma unroll
          for (int c = 0; c < 4; ++c) ac[c] = mfma32(df[4 * kk + c], hb, kk ? ac[c] : f32x4{0.f, 0.f, 0.f, 0.f});
        }
        publish_cols(rA, A_P2 + ((2 * layer + 1) * 144 + hk * 9 + sp - 7) * 256, ac, wave, g, j);
        STAMP(layer == 0 ? 12 : 24);
      } else {
        wait_vm<0>();
        bar();
      }
    }
    load_wq_frags();
    {
      PHASE();
      if (layer == 0) gather_cols<1>(rA, 1, tref + DLY_X2, wave, lane);
      if (layer == 1) {
        if ((b & 15) == 0 && hk < 9 && wave == 0) final_cols(rA, hk, tref + DLY_X2, lane);
        GAS u64* ep_ptr = (GAS u64*)(ws + WS_EPOCH);
        asm volatile("" : "+s"(ep_lo), "+s"(ep_hi));
        if (wave == 0 && b == 0 && lane == 0)
          __hip_atomic_store(ep_ptr, ((u64)ep_hi << 32) | ep_lo, __ATOMIC_RELAXED, __HIP_MEMORY_SCOPE_AGENT);
        STAMP(26);
        STAMP_DUMP();
        break;
      }
      tref = now();
      layer = 1;
    }
  }
}

extern "C" void kernel_launch(void* const* d_in, const int* in_sizes, int n_in, void* d_out, int out_size, void* d_ws,
                              size_t ws_size, hipStream_t stream) {
  Params p;
  p.x = (const u16*)d_in[0]; p.k = (const u16*)d_in[1]; p.v = (const u16*)d_in[2];
  p.wq = (const u16*)d_in[3]; p.wo = (const u16*)d_in[4];
  p.wg = (const u16*)d_in[6]; p.wu = (const u16*)d_in[7]; p.wd = (const u16*)d_in[8];
  p.out = (u16*)d_out; p.ws = (unsigned char*)d_ws;
  hipLaunchKernelGGL(fused_decode, dim3(NWG), dim3(256), 0, stream, p);
}
```
